```python
import torch

def _softmax(x, axis=-1):
    return torch.softmax(x, dim=axis)
def _gelu(x):
    return torch.nn.functional.gelu(x, approximate="tanh")
def _silu(x):
    return torch.nn.functional.silu(x)
def _sigmoid(x):
    return torch.sigmoid(x)
def _layernorm(x, axis=-1, eps=1e-5):
    m = x.mean(dim=axis, keepdim=True)
    v = x.var(dim=axis, keepdim=True, unbiased=False)
    return (x - m) / torch.sqrt(v + eps)
def _scatter_add(tgt, idx, src, axis=0):
    return tgt.clone().index_add_(axis, idx, src)
def _segment_sum(src, seg, n):
    out = torch.zeros((n,) + src.shape[1:], dtype=src.dtype)
    return out.index_add_(0, seg, src)
def _pad(x, pw):
    flat = tuple(v for pair in reversed(pw) for v in pair)
    return torch.nn.functional.pad(x, flat)

kernel_name = 'parallel_sum__ssm_conv_s4__dense_activation_squared_relu__prompt'

def setup_inputs():

    _w = (lambda a: (lambda t: t.to(torch.bfloat16) if t.is_floating_point() else t)(torch.as_tensor(a)))
    def _A_setup():
        B, L, D, N = 1, 2048, 448, 256
        A_re = -(lambda _x: torch.exp(torch.as_tensor(_x)))((lambda *s: torch.randn(*s, dtype=torch.bfloat16) * 3.2)(D, N))
        A_im = torch.pi * (lambda *s: torch.randn(*s, dtype=torch.bfloat16) * 0.2)(D, N)
        B_re = (lambda *s: torch.randn(*s, dtype=torch.bfloat16) * 0.4)(D, N)
        B_im = (lambda *s: torch.randn(*s, dtype=torch.bfloat16) * 0.4)(D, N)
        C_re = (lambda *s: torch.randn(*s, dtype=torch.bfloat16) * 0.4)(D, N)
        C_im = (lambda *s: torch.randn(*s, dtype=torch.bfloat16) * 0.4)(D, N)
        log_dt = (lambda *s: torch.randn(*s, dtype=torch.bfloat16) * 0.2)(D) * 0.5 - 4.0
        Dskip = (lambda *s: torch.randn(*s, dtype=torch.bfloat16) * 0.4)(D)
        return {
            "u": _w((lambda *s: torch.randn(*s, dtype=torch.bfloat16) * 0.05)(B, L, D)),
            "A_re": _w(A_re),
            "A_im": _w(A_im),
            "B_re": _w(B_re),
            "B_im": _w(B_im),
            "C_re": _w(C_re),
            "C_im": _w(C_im),
            "log_dt": _w(log_dt),
            "Dskip": _w(Dskip),
        }
    _A_in = _A_setup()

    def _B_setup():
        B, S, D, F = 1, 2048, 448, 1792
        return {
            "x": _w((lambda *s: torch.randn(*s, dtype=torch.bfloat16) * 0.05)(B, S, D)),
            "w1": _w((lambda *s: torch.randn(*s, dtype=torch.bfloat16) * 0.05)(D, F)),
            "b1": _w((lambda *s: torch.randn(*s, dtype=torch.bfloat16) * 0.2)(F)),
            "w2": _w((lambda *s: torch.randn(*s, dtype=torch.bfloat16) * 0.05)(F, D)),
            "b2": _w((lambda *s: torch.randn(*s, dtype=torch.bfloat16) * 0.8)(D)),
        }
    _B_in = _B_setup()

    return {
        "A_u": _A_in["u"],
        "A_A_re": _A_in["A_re"],
        "A_A_im": _A_in["A_im"],
        "A_B_re": _A_in["B_re"],
        "A_B_im": _A_in["B_im"],
        "A_C_re": _A_in["C_re"],
        "A_C_im": _A_in["C_im"],
        "A_log_dt": _A_in["log_dt"],
        "A_Dskip": _A_in["Dskip"],
        "B_x": _B_in["x"],
        "B_w1": _B_in["w1"],
        "B_b1": _B_in["b1"],
        "B_w2": _B_in["w2"],
        "B_b2": _B_in["b2"],
    }

def reference(A_u, A_A_re, A_A_im, A_B_re, A_B_im, A_C_re, A_C_im, A_log_dt, A_Dskip, B_x, B_w1, B_b1, B_w2, B_b2):
    def _A_ref(u, A_re, A_im, B_re, B_im, C_re, C_im, log_dt, Dskip):
        Bsz, L, D = u.shape
        dt = torch.exp(log_dt)[:, None]
        A = A_re + 1j * A_im
        Bc = B_re + 1j * B_im
        Cc = C_re + 1j * C_im
        logdA = dt * A
        dB = dt * Bc
        l = (lambda x, dt: x.to(dt))(torch.arange(L), A_re.dtype)
        P = torch.exp(logdA[:, :, None] * l[None, None, :])
        K = torch.einsum("dn,dnl,dn->dl", Cc, P, dB).real
        L2 = 2 * L
        Kp = _pad(K, ((0, 0), (0, L)))
        up = (lambda x, *d: x.permute(*d))(u, 0, 2, 1)
        up = _pad(up, ((0, 0), (0, 0), (0, L)))
        Kf = (lambda x, axes=(-2,-1): torch.fft.rfft2(x.float(), dim=axes))(Kp[:, None, :], axes=(-2, -1))
        Uf = (lambda x, axes=(-2,-1): torch.fft.rfft2(x.float(), dim=axes))(up[:, :, None, :], axes=(-2, -1))
        Yf = Uf * Kf[None, :, :, :]
        y = (lambda x, s, axes=(-2,-1): torch.fft.irfft2(x, s=s, dim=axes).to(torch.bfloat16))(Yf, (1, L2), axes=(-2, -1))[:, :, 0, :L]
        y = (lambda x, *d: x.permute(*d))(y, 0, 2, 1) + u * Dskip[None, None, :]
        return y

    def _B_ref(x, w1, b1, w2, b2):
        h = torch.einsum("bsd,df->bsf", x, w1) + b1
        zero = torch.zeros_like(h)
        r = torch.where(h > zero, h, zero)
        h = r * r
        out = torch.einsum("bsf,fd->bsd", h, w2) + b2
        return out

    x = A_u
    xn = _layernorm(x, axis=-1)
    return x + _A_ref(xn, A_A_re, A_A_im, A_B_re, A_B_im, A_C_re, A_C_im, A_log_dt, A_Dskip) + _B_ref(xn, B_w1, B_b1, B_w2, B_b2)
```

```cpp
#include <hip/hip_runtime.h>
#include <hip/hip_bf16.h>
#include <stdint.h>
#define ROUND_L 0
typedef unsigned short u16;
constexpr int L=2048, D=448, N=256, F=1792;
__device__ __forceinline__ float bf2f(u16 v){ return __uint_as_float(((unsigned)v)<<16); }
__device__ __forceinline__ u16 f2bf(float f){ unsigned u=__float_as_uint(f); u += 0x7fffu + ((u>>16)&1u); return (u16)(u>>16); }
__device__ __forceinline__ float rbf(float f){ return bf2f(f2bf(f)); }

__global__ void k_ln(const u16* x, u16* xn){
  int row=blockIdx.x; int lane=threadIdx.x;
  float v[7]; float s=0;
  for(int i=0;i<7;i++){ v[i]=bf2f(x[row*D+lane+64*i]); s+=v[i]; }
  for(int o=32;o;o>>=1) s+=__shfl_xor(s,o);
  float mean=s/D; float q=0;
  for(int i=0;i<7;i++){ float d=v[i]-mean; q+=d*d; }
  for(int o=32;o;o>>=1) q+=__shfl_xor(q,o);
  float m=rbf(mean); float var=rbf(q/D); float sd=rbf(sqrtf(rbf(var+1e-5f)));
  for(int i=0;i<7;i++) xn[row*D+lane+64*i]=f2bf(rbf(v[i]-m)/sd);
}
__global__ void k_kgen(const u16* Ar,const u16* Ai,const u16* Br,const u16* Bi,const u16* Cr,const u16* Ci,const u16* ldt,float* K){
  int d=blockIdx.x; __shared__ float sa[N],sb[N],scr[N],sci[N];
  int t=threadIdx.x;
  float dt=rbf(expf(bf2f(ldt[d])));
  { float ar=bf2f(Ar[d*N+t]), ai=bf2f(Ai[d*N+t]); sa[t]=dt*ar; sb[t]=dt*ai;
    float br=dt*bf2f(Br[d*N+t]), bi=dt*bf2f(Bi[d*N+t]); float cr=bf2f(Cr[d*N+t]), ci=bf2f(Ci[d*N+t]);
    scr[t]=cr*br-ci*bi; sci[t]=cr*bi+ci*br; }
  __syncthreads();
  for(int l=t+256*blockIdx.y;l<L;l+=256*gridDim.y){
    float lf=ROUND_L? rbf((float)l) : (float)l; float acc=0;
    for(int n=0;n<N;n++){ float e=expf(sa[n]*lf); float s,c; sincosf(sb[n]*lf,&s,&c); acc+=e*(scr[n]*c-sci[n]*s); }
    K[d*L+l]=acc;
  }
}
__global__ void k_conv(const u16* xn,const float* K,float* y){
  int d=blockIdx.x; __shared__ float sk[L], su[L];
  for(int i=threadIdx.x;i<L;i+=256){ sk[i]=K[d*L+i]; su[i]=bf2f(xn[i*D+d]); }
  __syncthreads();
  for(int l=threadIdx.x;l<L;l+=256){ float a=0; for(int j=0;j<=l;j++) a+=sk[j]*su[l-j]; y[l*D+d]=a; }
}
__global__ void k_g1(const u16* xn,const u16* w1,const u16* b1,u16* h){
  int f=blockIdx.x*256+threadIdx.x; int r0=blockIdx.y*8; __shared__ float sx[8][D];
  for(int i=threadIdx.x;i<8*D;i+=256) sx[i/D][i%D]=bf2f(xn[r0*D+i]);
  __syncthreads();
  float a[8]={0,0,0,0,0,0,0,0};
  for(int k=0;k<D;k++){ float w=bf2f(w1[k*F+f]); for(int r=0;r<8;r++) a[r]+=sx[r][k]*w; }
  for(int r=0;r<8;r++){ float v=rbf(rbf(a[r])+bf2f(b1[f])); v=v>0?v:0; h[(r0+r)*F+f]=f2bf(v*v); }
}
__global__ void k_g2(const u16* h,const u16* w2,const u16* b2,const u16* x,const u16* xn,const float* y,const u16* dsk,u16* out){
  int c=threadIdx.x; int r0=blockIdx.x*4; __shared__ float sh[4][F];
  for(int i=threadIdx.x;i<4*F;i+=448) sh[i/F][i%F]=bf2f(h[r0*F+i]);
  __syncthreads();
  float a[4]={0,0,0,0};
  for(int k=0;k<F;k++){ float w=bf2f(w2[k*D+c]); for(int r=0;r<4;r++) a[r]+=sh[r][k]*w; }
  for(int r=0;r<4;r++){ int i=(r0+r)*D+c; float o=rbf(rbf(a[r])+bf2f(b2[c]));
    float ya=rbf(rbf(y[i])+rbf(bf2f(xn[i])*bf2f(dsk[c])));
    out[i]=f2bf(rbf(bf2f(x[i])+ya)+o); }
}
extern "C" void kernel_launch(void* const* d_in, const int* in_sizes, int n_in, void* d_out, int out_size, void* d_ws, size_t ws_size, hipStream_t stream){
  const u16* x=(const u16*)d_in[0];
  char* ws=(char*)d_ws; u16* xn=(u16*)ws; float* K=(float*)(ws+(4<<20)); float* y=(float*)(ws+(8<<20)); u16* h=(u16*)(ws+(16<<20));
  k_ln<<<L,64,0,stream>>>(x,xn);
  k_kgen<<<dim3(D,4),256,0,stream>>>((const u16*)d_in[1],(const u16*)d_in[2],(const u16*)d_in[3],(const u16*)d_in[4],(const u16*)d_in[5],(const u16*)d_in[6],(const u16*)d_in[7],K);
  k_conv<<<D,256,0,stream>>>(xn,K,y);
  k_g1<<<dim3(F/256,L/8),256,0,stream>>>(xn,(const u16*)d_in[10],(const u16*)d_in[11],h);
  k_g2<<<L/4,448,0,stream>>>(h,(const u16*)d_in[12],(const u16*)d_in[13],x,xn,y,(const u16*)d_in[8],(u16*)d_out);
}
```

```cpp
#define EXPT 0
#include <hip/hip_runtime.h>
#include <stdint.h>

namespace {

typedef unsigned short u16;
typedef unsigned u32;
typedef unsigned long long u64;
#define LAS __attribute__((address_space(3)))
#define GAS __attribute__((address_space(1)))
#define DI __device__ __forceinline__
typedef __attribute__((ext_vector_type(2))) u32 u32x2;
typedef __attribute__((ext_vector_type(4))) u32 u32x4;
typedef __attribute__((ext_vector_type(2))) float f32x2;
typedef __attribute__((ext_vector_type(4))) float f32x4;
typedef __attribute__((ext_vector_type(16))) float f32x16;
typedef __attribute__((ext_vector_type(4))) short s16x4;
typedef __attribute__((ext_vector_type(2))) __bf16 bf16x2;
typedef __attribute__((ext_vector_type(8))) __bf16 bf16x8;
typedef __amdgpu_buffer_rsrc_t rsrc_t;

constexpr int L = 2048, D = 448, NM = 256, F = 1792;

constexpr u32 WS_EPOCH = 0, WS_ST = 4096, WS_XF = 20480, WS_W2F = 24576, WS_HF = 32768, WS_YF = 40960;
constexpr u32 WS_XT = 1u << 20, WS_YA = 3u << 20, WS_W2T = 5u << 20, WS_H = 8u << 20, WS_END = 16u << 20;
#define STAMP_OFF (32u << 20)

constexpr u32 LW1A = 0, LSTG = 57344, STG_W = 4608, STG_R = 288;
constexpr u32 LKC = 0, KC_CH = 33280, KC_CP = 4160, LU = 66560, U_CH = 4608;
constexpr u32 LPG = 75776, LPS = 75792, LTAG = 75808, LCS = 76032, LSTAT = 78080, LB1 = 78592;
constexpr u32 LXS = 81920, XS_W = 20480, LY = 81920, LP = 81920, P_R = 272, P_W = 64 * P_R;
constexpr u32 LDS_BYTES = 163840;
constexpr u32 SPIN_CAP = 1u << 16;

#define FENCE() asm volatile("" ::: "memory")
#define WAITV(n) asm volatile("s_waitcnt vmcnt(" #n ")" ::: "memory")
#define EXIT() do { asm volatile("s_endpgm" ::: "memory"); __builtin_amdgcn_s_waitcnt(0); } while (0)
#define SCHED() __builtin_amdgcn_sched_barrier(0)
#define RLX_AGENT __ATOMIC_RELAXED, __HIP_MEMORY_SCOPE_AGENT

#define STAMP(k)
#define STAMP_DECL
#define STAMP_FLUSH()

DI float lo16(u32 d) { return __uint_as_float(d << 16); }
DI float hi16(u32 d) { return __uint_as_float(d & 0xffff0000u); }
DI u32 pk(float a, float b) { f32x2 v = {a, b}; return __builtin_bit_cast(u32, __builtin_convertvector(v, bf16x2)); }
DI float rbf(float a) { return lo16(pk(a, a)); }
DI f32x16 mfma32(u32x4 a, u32x4 b, f32x16 c) { return __builtin_amdgcn_mfma_f32_32x32x16_bf16(__builtin_bit_cast(bf16x8, a), __builtin_bit_cast(bf16x8, b), c, 0, 0, 0); }
DI f32x4 mfma16(u32x4 a, u32x4 b, f32x4 c) { return __builtin_amdgcn_mfma_f32_16x16x32_bf16(__builtin_bit_cast(bf16x8, a), __builtin_bit_cast(bf16x8, b), c, 0, 0, 0); }
template <class T> DI T ldl(LAS char* p) { return *(LAS T*)p; }
template <class T> DI void stl(LAS char* p, T v) { *(LAS T*)p = v; }
DI u32x4 trfrag(LAS char* p, u32 step) {
  u32x2 a = __builtin_bit_cast(u32x2, __builtin_amdgcn_ds_read_tr16_b64_v4i16((LAS s16x4*)p));
  u32x2 b = __builtin_bit_cast(u32x2, __builtin_amdgcn_ds_read_tr16_b64_v4i16((LAS s16x4*)(p + step)));
  return u32x4{a[0], a[1], b[0], b[1]};
}
DI void dma(u32 ldsaddr, u32 voff, rsrc_t rs, u32 soff) {
  asm volatile("s_mov_b32 m0, %0\n\ts_nop 0\n\tbuffer_load_dwordx4 %1, %2, %3 offen lds" ::"s"(ldsaddr), "v"(voff), "s"(rs), "s"(soff) : "memory");
}
DI u32x4 ldg16(rsrc_t rs, u32 voff, u32 soff) { return __builtin_amdgcn_raw_buffer_load_b128(rs, voff, soff, 16); }
DI void stg16(rsrc_t rs, u32 voff, u32 soff, u32x4 v) { __builtin_amdgcn_raw_buffer_store_b128(v, rs, voff, soff, 16); }
DI u32 ldflag(char* ws, u32 off) { return __hip_atomic_load((GAS u32*)(ws + off), RLX_AGENT); }
DI u64 ldflag2(char* ws, u32 off) { return __hip_atomic_load((GAS u64*)(ws + off), RLX_AGENT); }
DI void stflag(char* ws, u32 off, u32 v) { __hip_atomic_store((GAS u32*)(ws + off), v, RLX_AGENT); }
DI bool allof(bool p) { return __builtin_amdgcn_ballot_w64(!p) == 0; }

DI void post(LAS char* lds, u32 base, int w, u32 v) { FENCE(); *(volatile LAS u32*)(lds + base + 4 * w) = v; FENCE(); }
template <bool SLEEP> DI void await(LAS char* lds, u32 base, u32 v) {
  FENCE();
#pragma unroll 1
  for (u32 n = 0; n < (SPIN_CAP << 4); ++n) {
    u32x4 p = *(volatile LAS u32x4*)(lds + base);
    u32 m = min(min(p[0], p[1]), min(p[2], p[3]));
    if ((u32)__builtin_amdgcn_readfirstlane(m) >= v) break;
    if (SLEEP) __builtin_amdgcn_s_sleep(1);
  }
  FENCE();
}
template <int CTRL> DI float dppadd(float v) { return v + __int_as_float(__builtin_amdgcn_update_dpp(0, __float_as_int(v), CTRL, 0xf, 0xf, true)); }
DI float sum32(float v) {
  v = dppadd<0xB1>(v); v = dppadd<0x4E>(v); v = dppadd<0x141>(v); v = dppadd<0x140>(v);
  auto r = __builtin_amdgcn_permlane16_swap(__float_as_uint(v), __float_as_uint(v), false, false);
  return __uint_as_float(r[0]) + __uint_as_float(r[1]);
}
struct cpx { float r, i; };
DI cpx cmul(cpx a, cpx b) { return {__builtin_fmaf(a.r, b.r, -(a.i * b.i)), __builtin_fmaf(a.r, b.i, a.i * b.r)}; }
DI cpx cmulc(cpx a, cpx b) { return {__builtin_fmaf(a.r, b.r, a.i * b.i), __builtin_fmaf(a.i, b.r, -(a.r * b.i))}; }
DI cpx csq(cpx a) { float t = a.r * a.i; return {__builtin_fmaf(a.r, a.r, -(a.i * a.i)), t + t}; }
DI cpx cpow(float a2, float b2, float k) {
  float e = __builtin_amdgcn_exp2f(a2 * k), f = __builtin_amdgcn_fractf(b2 * k);
  return {e * __builtin_amdgcn_cosf(f), e * __builtin_amdgcn_sinf(f)};
}

__global__ __launch_bounds__(512, 2) void fused_kernel(
    const u16* __restrict__ x, const u16* __restrict__ pAr, const u16* __restrict__ pAi, const u16* __restrict__ pBr,
    const u16* __restrict__ pBi, const u16* __restrict__ pCr, const u16* __restrict__ pCi, const u16* __restrict__ pdt,
    const u16* __restrict__ pds, const u16* __restrict__ w1, const u16* __restrict__ b1, const u16* __restrict__ w2,
    const u16* __restrict__ b2, u16* __restrict__ out, char* ws, const char* bx) {
  __shared__ __attribute__((aligned(16))) char smem[LDS_BYTES];
  LAS char* lds = (LAS char*)smem;
  const u32 lb = (u32)(size_t)lds;
  const int lane = threadIdx.x & 63, wave = __builtin_amdgcn_readfirstlane(threadIdx.x >> 6);
  const int xcd = blockIdx.x & 7, rk = blockIdx.x >> 3;
  const bool mainwg = rk < 28;
  const int h = lane >> 5;
  STAMP_DECL
  STAMP(0);
  const rsrc_t rsW = __builtin_amdgcn_make_buffer_rsrc(ws, 0, WS_END, 0x00020000);
  const int g1 = (xcd >> 1) * 4 + rk / 7, j1 = (xcd & 1) * 7 + rk % 7;
  const int g2 = xcd * 2 + rk / 14, th = (rk % 14) / 7, jc = rk % 7;
  if (threadIdx.x < 8) stl<u32>(lds + LPG + 4 * threadIdx.x, 0u);

  const rsrc_t rs1 = __builtin_amdgcn_make_buffer_rsrc((void*)w1, 0, D * F * 2, 0x00020000);
  const rsrc_t rsx = __builtin_amdgcn_make_buffer_rsrc((void*)x, 0, L * D * 2, 0x00020000);
  const u32 vW = (lane >> 3) * 3584 + 16 * ((lane & 7) ^ (((lane >> 4) & 1) << 2));
  const u32 vX0 = (lane >> 3) * 896 + 16 * ((lane & 7) ^ (lane >> 4));
  const u32 vX1 = vX0 ^ 64;
  const u32 sX = (g1 * 128 + wave * 32) * 896, xs = LXS + wave * XS_W;
  const int ks = ((rk - 28) * 8 + xcd) * 4 + wave;
  if (wave < 4) {
    __builtin_amdgcn_s_setprio(2);
    if (mainwg) {
#pragma unroll 1
      for (int i = wave * 14; i < wave * 14 + 14; ++i) dma(lb + LW1A + i * 1024, vW, rs1, i * 8 * 3584 + j1 * 128);
    } else if (ks < 112) {
      const rsrc_t rs2 = __builtin_amdgcn_make_buffer_rsrc((void*)w2, 0, F * D * 2, 0x00020000);
#pragma unroll 1
      for (int i = 0; i < 14; ++i) dma(lb + xs + i * 1024, lane * 16, rs2, ks * 14336 + i * 1024);
    }
    asm volatile("s_waitcnt lgkmcnt(0)" ::: "memory");
    __builtin_amdgcn_s_barrier();
    if (mainwg) {
#pragma unroll 1
      for (int c = 0; c < 5; ++c)
#pragma unroll
        for (int i = 0; i < 4; ++i) dma(lb + xs + c * 4096 + i * 1024, (i & 1) ? vX1 : vX0, rsx, sX + i * 8 * 896 + c * 128);
    }
  }
  int role = wave;
  asm volatile("" : "+s"(role));
  if (role >= 4) {
  const int sw = wave - 4, cl = sw >> 1, hf2 = sw & 1;
  const int tg = xcd * 32 + rk;
  const int ch = mainwg ? xcd * 56 + rk * 2 + cl : 0;
  const u32 ep = ldflag(ws, WS_EPOCH);
  u32 xr[7];
  {
    const u32* xp = (const u32*)(x + (tg * 8 + sw * 2 + h) * D) + (lane & 31);
#pragma unroll
    for (int i = 0; i < 7; ++i) xr[i] = xp[32 * i];
  }
  const int po = ch * (NM / 2) + hf2 * 64 + lane;
  const u32 dAr = ((const u32*)pAr)[po], dAi = ((const u32*)pAi)[po], dBr = ((const u32*)pBr)[po], dBi = ((const u32*)pBi)[po],
            dCr = ((const u32*)pCr)[po], dCi = ((const u32*)pCi)[po];
  const u32 ldtv = pdt[ch], dskv = pds[ch];
  const u32 b1v = b1[(sw >> 1) * 896 + (mainwg ? j1 : 0) * 64 + (sw & 1) * 32 + (lane & 31)];
  SCHED();
  asm volatile("s_waitcnt lgkmcnt(0)" ::: "memory");
  __builtin_amdgcn_s_barrier();
  SCHED();
  const u32 tag = __builtin_amdgcn_readfirstlane(ep) * 1664525u + 1013904223u;
  STAMP(1);
  {
    float v[14], s = 0.f;
#pragma unroll
    for (int i = 0; i < 7; ++i) { v[2 * i] = lo16(xr[i]); v[2 * i + 1] = hi16(xr[i]); s += v[2 * i] + v[2 * i + 1]; }
    const float mean = sum32(s) * (1.0f / D);
    float q = 0.f;
#pragma unroll
    for (int i = 0; i < 14; ++i) { const float d = v[i] - mean; q = __builtin_fmaf(d, d, q); }
    const float var = rbf(sum32(q) * (1.0f / D));
    const float m = rbf(mean), sd = rbf(__builtin_amdgcn_sqrtf(rbf(var + 1e-5f)));
    asm volatile("" ::"v"(dAr), "v"(dAi), "v"(dBr), "v"(dBi), "v"(dCr), "v"(dCi), "v"(ldtv), "v"(dskv), "v"(b1v));
    if ((lane & 31) == 0)
      __hip_atomic_store((GAS u64*)(ws + WS_ST + (tg * 8 + sw * 2 + h) * 8), ((u64)tag << 32) | pk(m, sd), RLX_AGENT);
    const float rs = 1.0f / sd;
#pragma unroll
    for (int i = 0; i < 7; ++i) {
      const u32 d = pk(v[2 * i] - m, v[2 * i + 1] - m);
      auto r = __builtin_amdgcn_permlane32_swap(__float_as_uint(lo16(d) * rs), __float_as_uint(hi16(d) * rs), false, false);
      stl<u32>(lds + LSTG + sw * 1792 + (2 * ((lane & 31) + 32 * i) + h) * 4, pk(__uint_as_float(r[0]), __uint_as_float(r[1])));
    }
    if (sw == 0) stl<u32>(lds + LTAG, tag);
  }
  post(lds, LPS, sw, 1);
  await<false>(lds, LPS, 1);
  {
    u32x4 t[2];
#pragma unroll
    for (int i = 0; i < 2; ++i) {
      LAS char* p = lds + LSTG + (sw * 112 + i * 56 + min(lane, 55)) * 4;
      t[i] = u32x4{ldl<u32>(p), ldl<u32>(p + 1792), ldl<u32>(p + 3584), ldl<u32>(p + 5376)};
    }
    post(lds, LPS, sw, 2);
    if (lane < 56) {
      stg16(rsW, lane * 16, WS_XT + (tg * D + sw * 112) * 16, t[0]);
      stg16(rsW, lane * 16, WS_XT + (tg * D + sw * 112 + 56) * 16, t[1]);
    }
  }
  STAMP(2);
  if (!mainwg) {
    WAITV(0);
    if (lane == 0) stflag(ws, WS_XF + (sw * 256 + tg) * 4, tag);
    STAMP(3);
    STAMP_FLUSH();
    EXIT();
  }
  const float dt = rbf(expf(lo16(ldtv)));
  LAS char* stg = lds + LSTG + sw * STG_W;
  f32x4 k00 = {0, 0, 0, 0}, k01 = k00, k10 = k00, k11 = k00;
  const u32 stsrc = WS_ST + (g1 * 128 + sw * 32 + (lane & 31)) * 8;
#pragma unroll 1
  for (int pass = 0; pass < 2; ++pass) {
    const int sh = pass ? 0 : 16;
    auto up = [&](u32 d) { return __uint_as_float((d << sh) & 0xffff0000u); };
    const float a2 = (dt * up(dAr)) * 1.44269504088896f, b2r = (dt * up(dAi)) * 0.15915494309189535f;
    const cpx z1 = cpow(a2, b2r, 1.f), z32 = cpow(a2, b2r, 32.f), z256 = cpow(a2, b2r, 256.f);
    const cpx cc = cmul(cpx{up(dCr), up(dCi)}, cpx{dt * up(dBr), dt * up(dBi)});
    const cpx z2 = csq(z1), z4 = csq(z2), z8 = csq(z4), z16 = csq(z8);
    LAS char* wp = stg + lane * 4;
    LAS char* rp = stg + (lane & 15) * STG_R + (lane >> 4) * 16;
    auto put = [&](int row, cpx v) { stl<u32>(wp + row * STG_R, pk(v.r, v.i)); };
    cpx b[4] = {cpx{1.f, 0.f}, z8, z16, cmul(z16, z8)};
    if (pass == 0) await<false>(lds, LPS, 2);
    FENCE();
    u32x4 fb0[4], fb1[4], fa[4];
#pragma unroll
    for (int i = 0; i < 4; ++i)
#pragma unroll
      for (int c = 0; c < 4; ++c) { put(4 * i + c, b[c]); b[c] = (i == 0 && c == 0) ? z32 : cmul(b[c], z32); }
    FENCE();
#pragma unroll
    for (int s = 0; s < 4; ++s) fb0[s] = ldl<u32x4>(rp + s * 64);
    FENCE();
#pragma unroll
    for (int i = 0; i < 4; ++i)
#pragma unroll
      for (int c = 0; c < 4; ++c) { put(4 * i + c, b[c]); if (i < 3) b[c] = cmul(b[c], z32); }
    FENCE();
#pragma unroll
    for (int s = 0; s < 4; ++s) fb1[s] = ldl<u32x4>(rp + s * 64);
    FENCE();
    u64 stg64 = 0;
    if (pass == 0) stg64 = ldflag2(ws, stsrc);
    cpx a[8];
    a[0] = cpx{cc.r, -cc.i};
    a[1] = cmulc(a[0], z1);
#pragma unroll
    for (int i = 2; i < 8; ++i) a[i] = cmulc(a[i - 2], z2);
#pragma unroll
    for (int i = 0; i < 8; ++i) put(i, a[i]);
    cpx e0 = cmulc(a[0], z256), e2 = cmulc(a[2], z256), e4 = cmulc(a[4], z256), e6 = cmulc(a[6], z256);
    put(8, e0); put(9, e2); put(10, e4); put(11, e6);
    e0 = cmulc(e0, z256); e4 = cmulc(e4, z256);
    put(12, e0); put(13, e4);
    e0 = cmulc(e0, z256); e4 = cmulc(e4, z256);
    put(14, e0); put(15, e4);
    FENCE();
#pragma unroll
    for (int s = 0; s < 4; ++s) fa[s] = ldl<u32x4>(rp + s * 64);
    FENCE();
#pragma unroll
    for (int i = 0; i < 5; ++i) { e0 = cmulc(e0, z256); put(i, e0); }
#pragma unroll
    for (int s = 0; s < 4; ++s) { k00 = mfma16(fa[s], fb0[s], k00); k01 = mfma16(fa[s], fb1[s], k01); }
    FENCE();
#pragma unroll
    for (int s = 0; s < 4; ++s) fa[s] = ldl<u32x4>(rp + s * 64);
    FENCE();
#pragma unroll
    for (int s = 0; s < 4; ++s) { k10 = mfma16(fa[s], fb0[s], k10); k11 = mfma16(fa[s], fb1[s], k11); }
    if (pass == 0) {
      WAITV(0);
      if (lane == 0) stflag(ws, WS_XF + (sw * 256 + tg) * 4, tag);
_Pragma("unroll 1")
      for (u32 n = 0; n < SPIN_CAP; ++n) {
        if (allof((u32)(stg64 >> 32) == tag)) break;
        stg64 = ldflag2(ws, stsrc);
      }
      if (lane < 32) {
        stl<u32>(lds + LSTAT + (sw * 32 + lane) * 4, (u32)stg64);
        stl<u32>(lds + LB1 + (sw * 32 + lane) * 4, b1v);
      }
      post(lds, LPS, sw, 3);
      STAMP(3);
    }
  }
  STAMP(4);
  {
    const int grp = lane >> 4, col = lane & 15;
    const u32 a0 = grp < 2 ? (8 * col + 4 * grp) * 4 : grp == 2 ? (256 + 4 * col) * 4 : (384 + 2 * col) * 4;
    const u32 d01 = grp == 3 ? 256 : 8, dbt = grp < 2 ? 512 : grp == 2 ? 256 : 128;
    stl<f32x2>(stg + a0, f32x2{k00[0], k00[1]}); stl<f32x2>(stg + a0 + d01, f32x2{k00[2], k00[3]});
    stl<f32x2>(stg + a0 + dbt, f32x2{k01[0], k01[1]}); stl<f32x2>(stg + a0 + dbt + d01, f32x2{k01[2], k01[3]});
    if (lane < 16) {
#pragma unroll
      for (int r = 0; r < 4; ++r) { stl<float>(stg + (512 + 32 * r + col) * 4, k10[r]); stl<float>(stg + (528 + 32 * r + col) * 4, k11[r]); }
    }
    if (lane == 16) stl<float>(stg + 640 * 4, k10[0]);
  }
  post(lds, LPS, sw, 4);
  {
    const u32 wfl = WS_W2F + (sw * 32 + (lane & 31)) * 4;
_Pragma("unroll 1")
    for (u32 n = 0; n < SPIN_CAP; ++n)
      if (allof((lane & 31) >= 28 || ldflag(ws, wfl) == tag)) break;
  }
  post(lds, LPS, sw, 5);
  STAMP(5);
  u32x4 uv[2];
  {
    const u32 xfl = WS_XF + ((ch / 112) * 256 + hf2 * 128 + lane) * 4;
_Pragma("unroll 1")
    for (u32 n = 0; n < SPIN_CAP; ++n) {
      const u32 f0 = ldflag(ws, xfl), f1 = ldflag(ws, xfl + 256);
      if (allof(f0 == tag && f1 == tag)) break;
    }
    FENCE();
    uv[0] = ldg16(rsW, lane * (D * 16), WS_XT + ((hf2 * 128) * D + ch) * 16);
    uv[1] = ldg16(rsW, lane * (D * 16), WS_XT + ((hf2 * 128 + 64) * D + ch) * 16);
  }
  STAMP(6);
  await<true>(lds, LPS, 4);
  await<true>(lds, LPG, 2);
  LAS char* kc = lds + LKC + cl * KC_CH;
  const int n0 = hf2 * 64 + lane, n1 = n0 + 128;
  {
    LAS char* vp = lds + LSTG + cl * 2 * STG_W;
    auto val = [&](int l) {
      const int bi = (int)(pk((float)l, 0.f)) - (0x4300 - 128);
      const int idx = l < 256 ? l : bi;
      return ldl<float>(vp + idx * 4) + ldl<float>(vp + STG_W + idx * 4);
    };
    float kv[8];
#pragma unroll
    for (int p = 0; p < 8; ++p) kv[p] = val(8 * n0 + p);
    if (n0 == 0) kv[0] += lo16(dskv);
    stl<u32x4>(kc + (n0 + 1) * 16, u32x4{pk(kv[7], kv[6]), pk(kv[5], kv[4]), pk(kv[3], kv[2]), pk(kv[1], kv[0])});
    LAS char* v3 = vp + (512 + n0) * 4;
    const float va = ldl<float>(v3) + ldl<float>(v3 + STG_W), vc = ldl<float>(v3 + 4) + ldl<float>(v3 + STG_W + 4);
    const u32 aa = pk(va, va);
    stl<u32x4>(kc + (n1 + 1) * 16, u32x4{pk(vc, vc), pk(vc, (lane & 1) ? vc : va), aa, aa});
    if (hf2 == 0 && lane == 0) stl<u32x4>(kc, u32x4{0u, 0u, 0u, 0u});
  }
  post(lds, LPS, sw, 6);
  await<false>(lds, LPS, 6);
  STAMP(7);
#pragma unroll
  for (int it = 0; it < 2; ++it) {
    LAS char* p = kc + (it ? n1 : n0) * 16;
    const u32x4 cu = ldl<u32x4>(p + 16), pv = ldl<u32x4>(p);
    const u32 d[8] = {cu[0], cu[1], cu[2], cu[3], pv[0], pv[1], pv[2], pv[3]};
    u32 o[7];
#pragma unroll
    for (int i = 0; i < 7; ++i) o[i] = __builtin_amdgcn_alignbit(d[i + 1], d[i], 16);
#pragma unroll
    for (int s = 1; s < 8; ++s) {
      const int m = s >> 1;
      stl<u32x4>(p + 16 + s * KC_CP, (s & 1) ? u32x4{o[m], o[m + 1], o[m + 2], o[m + 3]} : u32x4{d[m], d[m + 1], d[m + 2], d[m + 3]});
    }
  }
  LAS char* ub = lds + LU + cl * U_CH;
  if (hf2 == 0) {
    if (lane < 8) stl<u32x4>(kc + lane * KC_CP, u32x4{0u, 0u, 0u, 0u});
    if (lane < 32) stl<u32x4>(ub + lane * 16, u32x4{0u, 0u, 0u, 0u});
  }
  stl<u32x4>(ub + 512 + (hf2 * 128 + lane) * 16, uv[0]);
  stl<u32x4>(ub + 512 + (hf2 * 128 + 64 + lane) * 16, uv[1]);
  post(lds, LPS, sw, 7);
  await<false>(lds, LPS, 7);
  STAMP(8);
  if (blockIdx.x == 0 && sw == 0 && lane == 0) stflag(ws, WS_EPOCH, tag);
  f32x4 y[8];
#pragma unroll
  for (int q = 0; q < 8; ++q) y[q] = f32x4{0, 0, 0, 0};
  {
    const int kq = lane >> 4;
    LAS char* ka = kc + (7 - (lane & 7)) * KC_CP + (16 * hf2 + 3 + ((lane >> 3) & 1) - kq) * 16;
    LAS char* ua = ub + 512 + (16 * (lane & 15) - 128 * hf2 - 16 + 8 * kq) * 2;
#pragma unroll
    for (int m = 0; m < 4; ++m) {
      u32x4 A[8], B[8];
#pragma unroll
      for (int p = 0; p < 8; ++p) { A[p] = ldl<u32x4>(ka + m * 64 + p * 512); B[p] = ldl<u32x4>(ua - m * 64 + p * 512); }
#pragma unroll
      for (int p = 0; p < 8; ++p)
#pragma unroll
        for (int q = 0; q + p < 8; ++q) y[p + q] = mfma16(A[p], B[q], y[p + q]);
    }
  }
  STAMP(9);
  await<true>(lds, LPG, 5);
#pragma unroll
  for (int q = 0; q < 8; ++q) stl<f32x4>(lds + LY + sw * 8192 + (256 * q + 16 * (lane & 15) + 4 * (lane >> 4)) * 4, y[q]);
  post(lds, LPS, sw, 8);
  await<false>(lds, LPS, 8);
  {
    u32x4 o[2];
#pragma unroll
    for (int it = 0; it < 2; ++it) {
      LAS char* p = lds + LY + cl * 16384 + (hf2 * 1024 + it * 512 + lane * 8) * 4;
      const f32x4 a = ldl<f32x4>(p) + ldl<f32x4>(p + 8192), b = ldl<f32x4>(p + 16) + ldl<f32x4>(p + 8192 + 16);
      o[it] = u32x4{pk(a[0], a[1]), pk(a[2], a[3]), pk(b[0], b[1]), pk(b[2], b[3])};
    }
    post(lds, LPS, sw, 9);
    stg16(rsW, lane * 16, WS_YA + (ch * L + hf2 * 1024) * 2, o[0]);
    stg16(rsW, lane * 16, WS_YA + (ch * L + hf2 * 1024 + 512) * 2, o[1]);
  }
  STAMP(10);
  WAITV(0);
  if (lane == 0) stflag(ws, WS_YF + (hf2 * 512 + ch) * 4, tag);
  STAMP(11);
  const int tok0 = g2 * 128 + th * 64 + sw * 16, oc = lane & 7;
  float bb[2][8], s2[2];
  {
    u32x4 xv[2];
    u64 sv[2];
#pragma unroll
    for (int it = 0; it < 2; ++it) xv[it] = *(const u32x4*)(x + (tok0 + it * 8 + (lane >> 3)) * D + jc * 64 + oc * 8);
    const u32x4 bv = *(const u32x4*)(b2 + jc * 64 + oc * 8);
    const u32 yfl = WS_YF + ((g2 >> 3) * 512 + jc * 64 + lane) * 4, so = WS_ST + (tok0 + (lane >> 3)) * 8;
_Pragma("unroll 1")
    for (u32 n = 0; n < SPIN_CAP; ++n) {
      const u32 f = ldflag(ws, yfl);
      sv[0] = ldflag2(ws, so); sv[1] = ldflag2(ws, so + 64);
      if (allof(f == tag && (u32)(sv[0] >> 32) == tag && (u32)(sv[1] >> 32) == tag)) break;
      __builtin_amdgcn_s_sleep(2);
    }
    FENCE();
    STAMP(12);
    LAS char* yt = lds + sw * 2048;
#pragma unroll
    for (int i = 0; i < 2; ++i) {
      const u32x4 t = ldg16(rsW, (lane >> 1) * (L * 2) + (lane & 1) * 16, WS_YA + ((jc * 64 + i * 32) * L + tok0) * 2);
      stl<u32x4>(yt + i * 1024 + lane * 16, t);
    }
    FENCE();
#pragma unroll
    for (int it = 0; it < 2; ++it) {
      const float sd = hi16((u32)sv[it]);
      s2[it] = 1.0f / (sd * sd);
#pragma unroll
      for (int e = 0; e < 8; ++e) {
        const float ya = lo16(ldl<u16>(yt + (oc * 8 + e) * 32 + (it * 8 + (lane >> 3)) * 2));
        const float xe = (e & 1) ? hi16(xv[it][e >> 1]) : lo16(xv[it][e >> 1]);
        const float be = (e & 1) ? hi16(bv[e >> 1]) : lo16(bv[e >> 1]);
        bb[it][e] = (xe + ya) + be;
      }
    }
  }
  STAMP(13);
  await<false>(lds, LPG, 6);
  STAMP(14);
#pragma unroll
  for (int it = 0; it < 2; ++it) {
    LAS char* p = lds + LP + (sw * 16 + it * 8 + (lane >> 3)) * P_R + oc * 32;
    const f32x4 a = (ldl<f32x4>(p) + ldl<f32x4>(p + P_W)) + (ldl<f32x4>(p + 2 * P_W) + ldl<f32x4>(p + 3 * P_W));
    const f32x4 b = (ldl<f32x4>(p + 16) + ldl<f32x4>(p + P_W + 16)) + (ldl<f32x4>(p + 2 * P_W + 16) + ldl<f32x4>(p + 3 * P_W + 16));
    u32x4 o;
#pragma unroll
    for (int e = 0; e < 2; ++e) {
      o[e] = pk(__builtin_fmaf(a[2 * e], s2[it], bb[it][2 * e]), __builtin_fmaf(a[2 * e + 1], s2[it], bb[it][2 * e + 1]));
      o[2 + e] = pk(__builtin_fmaf(b[2 * e], s2[it], bb[it][4 + 2 * e]), __builtin_fmaf(b[2 * e + 1], s2[it], bb[it][5 + 2 * e]));
    }
    *(u32x4*)(out + (tok0 + it * 8 + (lane >> 3)) * D + jc * 64 + oc * 8) = o;
  }
  STAMP(15);

  STAMP_FLUSH();
  EXIT();
  }
  int wv = wave;
  asm volatile("" : "+s"(wv));
  {
    if (!mainwg) {
      if (ks < 112) {
        const int sg = ks / 56, jj = (ks % 56) >> 2;
        WAITV(0);
        STAMP(1);
        LAS char* a = lds + xs + (4 * h + ((lane & 15) >> 2)) * 896 + ((lane >> 4) & 1) * 32 + (lane & 3) * 8;
#pragma unroll
        for (int t = 0; t < 14; ++t) {
          u32x4 f = trfrag(a + t * 64, 8 * 896);
          stg16(rsW, lane * 16, WS_W2T + (((((t >> 1) * 2 + sg) * 4 + wv) * 14 + jj) * 2 + (t & 1)) * 1024, f);
        }
        await<true>(lds, LPS, 1);
        const u32 tag = ldl<u32>(lds + LTAG);
        STAMP(2);
        WAITV(0);
        if (lane == 0) stflag(ws, WS_W2F + (wv * 32 + sg * 14 + jj) * 4, tag);
        STAMP(3);
      }
      STAMP_FLUSH();
      EXIT();
    }
    STAMP(1);
    const int q4 = (lane & 15) >> 2;
    const u32 aW = (8 * h + q4) * 128 + ((((lane >> 4) & 1) * 32 + (lane & 3) * 8) ^ ((q4 >> 1) * 64));
    const u32 aX = (lane & 31) * 128 + 16 * (h ^ ((lane >> 1) & 7));
    const u32 selw = ((lane & 15) == ((lane >> 4) & 1)) ? 0x3F803F80u : 0u;
    const u32x4 sel = {selw, selw, selw, selw};
    WAITV(20);
    STAMP(2);
    {
      f32x4 c0 = {0, 0, 0, 0}, c1 = {0, 0, 0, 0};
#pragma unroll
      for (int s = 0; s < 7; ++s) {
        LAS char* p = lds + LW1A + (wv * 7 + s) * 2048;
        c0 = mfma16(sel, trfrag(p + aW, 512), c0);
        c1 = mfma16(sel, trfrag(p + (aW ^ 64), 512), c1);
      }
      if (lane < 16) {
        LAS char* p = lds + LCS + wv * 256 + lane * 4;
        stl<float>(p, c0[0]); stl<float>(p + 64, c0[1]); stl<float>(p + 128, c1[0]); stl<float>(p + 192, c1[1]);
      }
    }
    post(lds, LPG, wv, 1);
    await<false>(lds, LPG, 1);
    STAMP(3);
    u32x4 xf[28];
    f32x16 acc0, acc1;
#pragma unroll
    for (int i = 0; i < 16; ++i) { acc0[i] = 0.f; acc1[i] = 0.f; }
    u32x4 af[2][8];
#pragma unroll
    for (int s = 0; s < 4; ++s) { af[0][2 * s] = trfrag(lds + LW1A + s * 2048 + aW, 512); af[0][2 * s + 1] = trfrag(lds + LW1A + s * 2048 + (aW ^ 64), 512); }
#pragma unroll
    for (int c = 0; c < 7; ++c) {
      if (c < 6) WAITV(16); else WAITV(14);
      const u32 slot = xs + (c % 5) * 4096;
#pragma unroll
      for (int s = 0; s < 4; ++s) xf[4 * c + s] = ldl<u32x4>(lds + slot + (aX ^ (32 * s)));
      if (c < 6) {
#pragma unroll
        for (int s = 0; s < 4; ++s) {
          LAS char* p = lds + LW1A + (4 * c + 4 + s) * 2048;
          af[(c + 1) & 1][2 * s] = trfrag(p + aW, 512); af[(c + 1) & 1][2 * s + 1] = trfrag(p + (aW ^ 64), 512);
        }
      }
      asm volatile("" : "+v"(xf[4 * c]), "+v"(xf[4 * c + 1]), "+v"(xf[4 * c + 2]), "+v"(xf[4 * c + 3]));
      if (c < 2) {
#pragma unroll
        for (int i = 0; i < 4; ++i) dma(lb + slot + i * 1024, (i & 1) ? vX1 : vX0, rsx, sX + i * 8 * 896 + (c + 5) * 128);
      } else if (c < 6) {
#pragma unroll
        for (int i = 0; i < (c < 5 ? 4 : 2); ++i) {
          const int n = 2 * (c - 2) + (i >> 1);
          dma(lb + slot + i * 1024, vW, rs1, ((4 * n + wv) * 16 + (i & 1) * 8) * 3584 + 1792 + j1 * 128);
        }
      }
#pragma unroll
      for (int s = 0; s < 4; ++s) {
        acc0 = mfma32(af[c & 1][2 * s], xf[4 * c + s], acc0);
        acc1 = mfma32(af[c & 1][2 * s + 1], xf[4 * c + s], acc1);
      }
      SCHED();
    }
    post(lds, LPG, wv, 2);
    STAMP(4);
    await<false>(lds, LPS, 3);
    STAMP(5);
    const u32 tag = __builtin_amdgcn_readfirstlane(ldl<u32>(lds + LTAG));
    u32x4 bg0, bg1;
    {
      const u32 st = ldl<u32>(lds + LSTAT + (wv * 32 + (lane & 31)) * 4);
      const u32 nm = (st & 0xffffu) ^ 0x8000u;
      const u32 d0 = nm | (nm << 16), d1 = st >> 16;
      bg0 = u32x4{h ? 0u : d0, h ? 0u : d1, 0u, 0u};
      bg1 = u32x4{h ? d0 : 0u, h ? d1 : 0u, 0u, 0u};
    }
    auto aug = [&](int sg) {
      LAS char* p = lds + LCS + sg * 1024 + lane * 4;
      const float cs = (ldl<float>(p) + ldl<float>(p + 256)) + (ldl<float>(p + 512) + ldl<float>(p + 768));
      const float hi = rbf(cs);
      const u32x4 a = {pk(hi, cs - hi), ldl<u32>(lds + LB1 + sg * 256 + lane * 4), 0u, 0u};
      acc0 = mfma32(a, bg0, acc0);
      acc1 = mfma32(a, bg1, acc1);
    };
    const u32 hdst = WS_H + ((g1 * 2 + (wv >> 1)) * 8 * 14 + j1) * 2048 + (wv & 1) * 1024;
    auto epi = [&](int sg) {
#pragma unroll
      for (int kw = 0; kw < 4; ++kw) {
        u32x4 o;
#pragma unroll
        for (int e = 0; e < 4; ++e) {
          const int i = 8 * (kw & 1) + 2 * e;
          float a = (kw >> 1) ? acc1[i] : acc0[i], b = (kw >> 1) ? acc1[i + 1] : acc0[i + 1];
          a = fmaxf(a, 0.f); b = fmaxf(b, 0.f);
          o[e] = pk(a * a, b * b);
        }
        stg16(rsW, lane * 16, hdst + (sg * 4 + kw) * 14 * 2048, o);
      }
    };
    auto bptr = [&](int s) -> LAS char* { const int n = s >> 2; return lds + LXS + (s & 3) * XS_W + (n < 6 ? 8192 + 2048 * n : 0); };
    aug(0);
    WAITV(6);
    post(lds, LPG, wv, 3);
    f32x4 c0 = {0, 0, 0, 0}, c1 = {0, 0, 0, 0};
#pragma unroll
    for (int n = 0; n < 4; ++n) {
      LAS char* p = lds + xs + 8192 + 2048 * n;
      c0 = mfma16(sel, trfrag(p + aW, 512), c0);
      c1 = mfma16(sel, trfrag(p + (aW ^ 64), 512), c1);
    }
    epi(0);
    STAMP(6);
    await<false>(lds, LPG, 3);
    STAMP(7);
#pragma unroll
    for (int i = 0; i < 16; ++i) { acc0[i] = 0.f; acc1[i] = 0.f; }
    u32x4 wf[28][2];
    const u32 wsrc = WS_W2T + (jc * 8 + wv) * 14 * 2048;
#pragma unroll
    for (int s = 0; s < 4; ++s) { af[0][2 * s] = trfrag(bptr(s) + aW, 512); af[0][2 * s + 1] = trfrag(bptr(s) + (aW ^ 64), 512); }
#pragma unroll
    for (int c = 0; c < 7; ++c) {
      if (c == 2) {
        WAITV(4);
#pragma unroll
        for (int n = 4; n < 7; ++n) {
          LAS char* p = lds + xs + (n < 6 ? 8192 + 2048 * n : 0);
          c0 = mfma16(sel, trfrag(p + aW, 512), c0);
          c1 = mfma16(sel, trfrag(p + (aW ^ 64), 512), c1);
        }
        if (lane < 16) {
          LAS char* p = lds + LCS + 1024 + wv * 256 + lane * 4;
          stl<float>(p, c0[0]); stl<float>(p + 64, c0[1]); stl<float>(p + 128, c1[0]); stl<float>(p + 192, c1[1]);
        }
        post(lds, LPG, wv, 4);
      }
      if (c == 3) await<false>(lds, LPG, 4);
      if (c < 6) {
#pragma unroll
        for (int s = 0; s < 4; ++s) {
          LAS char* p = bptr(4 * c + 4 + s);
          af[(c + 1) & 1][2 * s] = trfrag(p + aW, 512); af[(c + 1) & 1][2 * s + 1] = trfrag(p + (aW ^ 64), 512);
        }
      }
      if (c == 5) {
        await<false>(lds, LPS, 5);
#pragma unroll
        for (int i = 0; i < 8; ++i) { wf[i][0] = ldg16(rsW, lane * 16, wsrc + i * 2048); wf[i][1] = ldg16(rsW, lane * 16, wsrc + i * 2048 + 1024); }
      }
      if (c == 6) {
        WAITV(16);
        if (lane == 0) stflag(ws, WS_HF + (((g1 * 2 + (wv >> 1)) * 2 + 0) * 32 + (wv & 1) * 16 + j1) * 4, tag);
      }
#pragma unroll
      for (int s = 0; s < 4; ++s) {
        acc0 = mfma32(af[c & 1][2 * s], xf[4 * c + s], acc0);
        acc1 = mfma32(af[c & 1][2 * s + 1], xf[4 * c + s], acc1);
      }
      SCHED();
    }
    post(lds, LPG, wv, 5);
    aug(1);
    STAMP(8);
    epi(1);
    SCHED();
#pragma unroll
    for (int i = 8; i < 14; ++i) { wf[i][0] = ldg16(rsW, lane * 16, wsrc + i * 2048); wf[i][1] = ldg16(rsW, lane * 16, wsrc + i * 2048 + 1024); }
    STAMP(9);
    WAITV(12);
    if (lane == 0) stflag(ws, WS_HF + (((g1 * 2 + (wv >> 1)) * 2 + 1) * 32 + (wv & 1) * 16 + j1) * 4, tag);
    STAMP(10);
    const u32 hfl = WS_HF + ((g2 * 2 + th) * 2) * 128 + (lane & 31) * 4;
    const bool hskip = (lane & 15) >= 14;
_Pragma("unroll 1")
    for (u32 n = 0; n < SPIN_CAP; ++n)
      if (allof(hskip || ldflag(ws, hfl) == tag)) break;
    FENCE();
    STAMP(11);
    const u32 hsrc = WS_H + ((g2 * 2 + th) * 8 + wv) * 14 * 2048;
    u32x4 hf[28][2];
    auto ldh = [&](int i) { const u32 o = hsrc + (i / 14) * 4 * 14 * 2048 + (i % 14) * 2048; hf[i][0] = ldg16(rsW, lane * 16, o); hf[i][1] = ldg16(rsW, lane * 16, o + 1024); };
    auto ldw = [&](int i) { const u32 o = wsrc + (i / 14) * 4 * 14 * 2048 + (i % 14) * 2048; wf[i][0] = ldg16(rsW, lane * 16, o); wf[i][1] = ldg16(rsW, lane * 16, o + 1024); };
    f32x16 o00, o01, o10, o11;
#pragma unroll
    for (int i = 0; i < 16; ++i) { o00[i] = 0.f; o01[i] = 0.f; o10[i] = 0.f; o11[i] = 0.f; }
#pragma unroll
    for (int i = 0; i < 4; ++i) ldh(i);
    SCHED();
    u32 fb = 0;
#pragma unroll
    for (int i = 0; i < 28; ++i) {
      o00 = mfma32(wf[i][0], hf[i][0], o00);
      o01 = mfma32(wf[i][0], hf[i][1], o01);
      o10 = mfma32(wf[i][1], hf[i][0], o10);
      o11 = mfma32(wf[i][1], hf[i][1], o11);
      SCHED();
      if (i < 5) { ldh(4 + 2 * i); ldh(5 + 2 * i); }
      if (i == 4) fb = ldflag(ws, hfl + 128);
      if (i >= 5 && i < 8) { ldw(14 + 2 * (i - 5)); ldw(15 + 2 * (i - 5)); }
      if (i == 8) {
        if (!allof(hskip || fb == tag))
_Pragma("unroll 1")
          for (u32 n = 0; n < SPIN_CAP; ++n)
            if (allof(hskip || ldflag(ws, hfl + 128) == tag)) break;
        FENCE();
        STAMP(12);
      }
      if (i >= 8 && i < 11) { ldh(14 + 2 * (i - 8)); ldh(15 + 2 * (i - 8)); }
      if (i >= 11 && i < 19) { ldw(20 + (i - 11)); ldh(20 + (i - 11)); }
      SCHED();
    }
    STAMP(13);
    await<false>(lds, LPS, 9);
    {
      LAS char* p = lds + LP + wv * P_W + (lane & 31) * P_R + h * 16;
#pragma unroll
      for (int qd = 0; qd < 4; ++qd) {
        stl<f32x4>(p + qd * 32, f32x4{o00[4 * qd], o00[4 * qd + 1], o00[4 * qd + 2], o00[4 * qd + 3]});
        stl<f32x4>(p + qd * 32 + 32 * P_R, f32x4{o01[4 * qd], o01[4 * qd + 1], o01[4 * qd + 2], o01[4 * qd + 3]});
        stl<f32x4>(p + qd * 32 + 128, f32x4{o10[4 * qd], o10[4 * qd + 1], o10[4 * qd + 2], o10[4 * qd + 3]});
        stl<f32x4>(p + qd * 32 + 128 + 32 * P_R, f32x4{o11[4 * qd], o11[4 * qd + 1], o11[4 * qd + 2], o11[4 * qd + 3]});
      }
    }
    post(lds, LPG, wv, 6);
    STAMP(14);
    STAMP_FLUSH();
    EXIT();
  }

}

}

extern "C" void kernel_launch(void* const* d_in, const int* in_sizes, int n_in, void* d_out, int out_size, void* d_ws,
                              size_t ws_size, hipStream_t stream) {
  fused_kernel<<<256, 512, 0, stream>>>((const u16*)d_in[0], (const u16*)d_in[1], (const u16*)d_in[2], (const u16*)d_in[3],
                                        (const u16*)d_in[4], (const u16*)d_in[5], (const u16*)d_in[6], (const u16*)d_in[7],
                                        (const u16*)d_in[8], (const u16*)d_in[10], (const u16*)d_in[11], (const u16*)d_in[12],
                                        (const u16*)d_in[13], (u16*)d_out, (char*)d_ws, (const char*)d_in[9]);
}
```

```cpp
#define EXPT 3
#include <hip/hip_runtime.h>
#include <stdint.h>

namespace {

typedef unsigned short u16;
typedef unsigned u32;
typedef unsigned long long u64;
#define LAS __attribute__((address_space(3)))
#define GAS __attribute__((address_space(1)))
#define DI __device__ __forceinline__
typedef __attribute__((ext_vector_type(2))) u32 u32x2;
typedef __attribute__((ext_vector_type(4))) u32 u32x4;
typedef __attribute__((ext_vector_type(2))) float f32x2;
typedef __attribute__((ext_vector_type(4))) float f32x4;
typedef __attribute__((ext_vector_type(16))) float f32x16;
typedef __attribute__((ext_vector_type(4))) short s16x4;
typedef __attribute__((ext_vector_type(2))) __bf16 bf16x2;
typedef __attribute__((ext_vector_type(8))) __bf16 bf16x8;
typedef __amdgpu_buffer_rsrc_t rsrc_t;

constexpr int L = 2048, D = 448, NM = 256, F = 1792;

constexpr u32 WS_EPOCH = 0, WS_ST = 4096, WS_XF = 20480, WS_W2F = 24576, WS_HF = 32768, WS_YF = 40960;
constexpr u32 WS_XT = 1u << 20, WS_YA = 3u << 20, WS_W2T = 5u << 20, WS_H = 8u << 20, WS_END = 16u << 20;
#define STAMP_OFF (32u << 20)

constexpr u32 LW1A = 0, LSTG = 57344, STG_W = 4608, STG_R = 288;
constexpr u32 LKC = 0, KC_CH = 33280, KC_CP = 4160, LU = 66560, U_CH = 4608;
constexpr u32 LPG = 75776, LPS = 75792, LTAG = 75808, LCS = 76032, LSTAT = 78080, LB1 = 78592;
constexpr u32 LXS = 81920, XS_W = 20480, LY = 81920, LP = 81920, P_R = 272, P_W = 64 * P_R;
constexpr u32 LDS_BYTES = 163840;
constexpr u32 SPIN_CAP = 1u << 16;

#define FENCE() asm volatile("" ::: "memory")
#define WAITV(n) asm volatile("s_waitcnt vmcnt(" #n ")" ::: "memory")
#define EXIT() do { asm volatile("s_endpgm" ::: "memory"); __builtin_amdgcn_s_waitcnt(0); } while (0)
#define SCHED() __builtin_amdgcn_sched_barrier(0)
#define RLX_AGENT __ATOMIC_RELAXED, __HIP_MEMORY_SCOPE_AGENT

#define STAMP(k)
#define STAMP_DECL
#define STAMP_FLUSH()

DI float lo16(u32 d) { return __uint_as_float(d << 16); }
DI float hi16(u32 d) { return __uint_as_float(d & 0xffff0000u); }
DI u32 pk(float a, float b) { f32x2 v = {a, b}; return __builtin_bit_cast(u32, __builtin_convertvector(v, bf16x2)); }
DI float rbf(float a) { return lo16(pk(a, a)); }
DI f32x16 mfma32(u32x4 a, u32x4 b, f32x16 c) { return __builtin_amdgcn_mfma_f32_32x32x16_bf16(__builtin_bit_cast(bf16x8, a), __builtin_bit_cast(bf16x8, b), c, 0, 0, 0); }
DI f32x4 mfma16(u32x4 a, u32x4 b, f32x4 c) { return __builtin_amdgcn_mfma_f32_16x16x32_bf16(__builtin_bit_cast(bf16x8, a), __builtin_bit_cast(bf16x8, b), c, 0, 0, 0); }
template <class T> DI T ldl(LAS char* p) { return *(LAS T*)p; }
template <class T> DI void stl(LAS char* p, T v) { *(LAS T*)p = v; }
DI u32x4 trfrag(LAS char* p, u32 step) {
  u32x2 a = __builtin_bit_cast(u32x2, __builtin_amdgcn_ds_read_tr16_b64_v4i16((LAS s16x4*)p));
  u32x2 b = __builtin_bit_cast(u32x2, __builtin_amdgcn_ds_read_tr16_b64_v4i16((LAS s16x4*)(p + step)));
  return u32x4{a[0], a[1], b[0], b[1]};
}
DI void dma(u32 ldsaddr, u32 voff, rsrc_t rs, u32 soff) {
  asm volatile("s_mov_b32 m0, %0\n\ts_nop 0\n\tbuffer_load_dwordx4 %1, %2, %3 offen lds" ::"s"(ldsaddr), "v"(voff), "s"(rs), "s"(soff) : "memory");
}
DI u32x4 ldg16(rsrc_t rs, u32 voff, u32 soff) { return __builtin_amdgcn_raw_buffer_load_b128(rs, voff, soff, 16); }
DI void stg16(rsrc_t rs, u32 voff, u32 soff, u32x4 v) { __builtin_amdgcn_raw_buffer_store_b128(v, rs, voff, soff, 16); }
DI u32 ldflag(char* ws, u32 off) { return __hip_atomic_load((GAS u32*)(ws + off), RLX_AGENT); }
DI u64 ldflag2(char* ws, u32 off) { return __hip_atomic_load((GAS u64*)(ws + off), RLX_AGENT); }
DI void stflag(char* ws, u32 off, u32 v) { __hip_atomic_store((GAS u32*)(ws + off), v, RLX_AGENT); }
DI bool allof(bool p) { return __builtin_amdgcn_ballot_w64(!p) == 0; }

DI void post(LAS char* lds, u32 base, int w, u32 v) { FENCE(); *(volatile LAS u32*)(lds + base + 4 * w) = v; FENCE(); }
template <bool SLEEP> DI void await(LAS char* lds, u32 base, u32 v) {
  FENCE();
#pragma unroll 1
  for (u32 n = 0; n < (SPIN_CAP << 4); ++n) {
    u32x4 p = *(volatile LAS u32x4*)(lds + base);
    u32 m = min(min(p[0], p[1]), min(p[2], p[3]));
    if ((u32)__builtin_amdgcn_readfirstlane(m) >= v) break;
    if (SLEEP) __builtin_amdgcn_s_sleep(1);
  }
  FENCE();
}
template <int CTRL> DI float dppadd(float v) { return v + __int_as_float(__builtin_amdgcn_update_dpp(0, __float_as_int(v), CTRL, 0xf, 0xf, true)); }
DI float sum32(float v) {
  v = dppadd<0xB1>(v); v = dppadd<0x4E>(v); v = dppadd<0x141>(v); v = dppadd<0x140>(v);
  auto r = __builtin_amdgcn_permlane16_swap(__float_as_uint(v), __float_as_uint(v), false, false);
  return __uint_as_float(r[0]) + __uint_as_float(r[1]);
}
struct cpx { float r, i; };
DI cpx cmul(cpx a, cpx b) { return {__builtin_fmaf(a.r, b.r, -(a.i * b.i)), __builtin_fmaf(a.r, b.i, a.i * b.r)}; }
DI cpx cmulc(cpx a, cpx b) { return {__builtin_fmaf(a.r, b.r, a.i * b.i), __builtin_fmaf(a.i, b.r, -(a.r * b.i))}; }
DI cpx csq(cpx a) { float t = a.r * a.i; return {__builtin_fmaf(a.r, a.r, -(a.i * a.i)), t + t}; }
DI cpx cpow(float a2, float b2, float k) {
  float e = __builtin_amdgcn_exp2f(a2 * k), f = __builtin_amdgcn_fractf(b2 * k);
  return {e * __builtin_amdgcn_cosf(f), e * __builtin_amdgcn_sinf(f)};
}

__global__ __launch_bounds__(512, 2) void fused_kernel(
    const u16* __restrict__ x, const u16* __restrict__ pAr, const u16* __restrict__ pAi, const u16* __restrict__ pBr,
    const u16* __restrict__ pBi, const u16* __restrict__ pCr, const u16* __restrict__ pCi, const u16* __restrict__ pdt,
    const u16* __restrict__ pds, const u16* __restrict__ w1, const u16* __restrict__ b1, const u16* __restrict__ w2,
    const u16* __restrict__ b2, u16* __restrict__ out, char* ws) {
  __shared__ __attribute__((aligned(16))) char smem[LDS_BYTES];
  LAS char* lds = (LAS char*)smem;
  const u32 lb = (u32)(size_t)lds;
  const int lane = threadIdx.x & 63, wave = __builtin_amdgcn_readfirstlane(threadIdx.x >> 6);
  const int xcd = blockIdx.x & 7, rk = blockIdx.x >> 3;
  const bool mainwg = rk < 28;
  const int h = lane >> 5;
  STAMP_DECL
  STAMP(0);
  const u64 tst = __builtin_amdgcn_s_memrealtime();
  const rsrc_t rsW = __builtin_amdgcn_make_buffer_rsrc(ws, 0, WS_END, 0x00020000);
  const int g1 = (xcd >> 1) * 4 + rk / 7, j1 = (xcd & 1) * 7 + rk % 7;
  const int g2 = xcd * 2 + rk / 14, th = (rk % 14) / 7, jc = rk % 7;
  if (threadIdx.x < 8) stl<u32>(lds + LPG + 4 * threadIdx.x, 0u);

  const rsrc_t rs1 = __builtin_amdgcn_make_buffer_rsrc((void*)w1, 0, D * F * 2, 0x00020000);
  const rsrc_t rsx = __builtin_amdgcn_make_buffer_rsrc((void*)x, 0, L * D * 2, 0x00020000);
  const u32 vW = (lane >> 3) * 3584 + 16 * ((lane & 7) ^ (((lane >> 4) & 1) << 2));
  const u32 vX0 = (lane >> 3) * 896 + 16 * ((lane & 7) ^ (lane >> 4));
  const u32 vX1 = vX0 ^ 64;
  const u32 sX = (g1 * 128 + wave * 32) * 896, xs = LXS + wave * XS_W;
  const int ks = ((rk - 28) * 8 + xcd) * 4 + wave;
  if (wave < 4) {
    __builtin_amdgcn_s_setprio(2);
    if (mainwg) {
#pragma unroll 1
      for (int i = wave * 14; i < wave * 14 + 14; ++i) dma(lb + LW1A + i * 1024, vW, rs1, i * 8 * 3584 + j1 * 128);
    } else if (ks < 112) {
      const rsrc_t rs2 = __builtin_amdgcn_make_buffer_rsrc((void*)w2, 0, F * D * 2, 0x00020000);
#pragma unroll 1
      for (int i = 0; i < 14; ++i) dma(lb + xs + i * 1024, lane * 16, rs2, ks * 14336 + i * 1024);
    }
    asm volatile("s_waitcnt lgkmcnt(0)" ::: "memory");
    __builtin_amdgcn_s_barrier();
    if (mainwg) {
#pragma unroll 1
      for (int c = 0; c < 5; ++c)
#pragma unroll
        for (int i = 0; i < 4; ++i) dma(lb + xs + c * 4096 + i * 1024, (i & 1) ? vX1 : vX0, rsx, sX + i * 8 * 896 + c * 128);
    }
  }
  int role = wave;
  asm volatile("" : "+s"(role));
  if (role >= 4) {
  const int sw = wave - 4, cl = sw >> 1, hf2 = sw & 1;
  const int tg = xcd * 32 + rk;
  const int ch = mainwg ? xcd * 56 + rk * 2 + cl : 0;
  const u32 ep = ldflag(ws, WS_EPOCH);
  u32 xr[7];
  {
    const u32* xp = (const u32*)(x + (tg * 8 + sw * 2 + h) * D) + (lane & 31);
#pragma unroll
    for (int i = 0; i < 7; ++i) xr[i] = xp[32 * i];
  }
  const int po = ch * (NM / 2) + hf2 * 64 + lane;
  const u32 dAr = ((const u32*)pAr)[po], dAi = ((const u32*)pAi)[po], dBr = ((const u32*)pBr)[po], dBi = ((const u32*)pBi)[po],
            dCr = ((const u32*)pCr)[po], dCi = ((const u32*)pCi)[po];
  const u32 ldtv = pdt[ch], dskv = pds[ch];
  const u32 b1v = b1[(sw >> 1) * 896 + (mainwg ? j1 : 0) * 64 + (sw & 1) * 32 + (lane & 31)];
  SCHED();
  asm volatile("s_waitcnt lgkmcnt(0)" ::: "memory");
  __builtin_amdgcn_s_barrier();
  SCHED();
  const u32 tag = __builtin_amdgcn_readfirstlane(ep) * 1664525u + 1013904223u;
  STAMP(1);
  {
    float v[14], s = 0.f;
#pragma unroll
    for (int i = 0; i < 7; ++i) { v[2 * i] = lo16(xr[i]); v[2 * i + 1] = hi16(xr[i]); s += v[2 * i] + v[2 * i + 1]; }
    const float mean = sum32(s) * (1.0f / D);
    float q = 0.f;
#pragma unroll
    for (int i = 0; i < 14; ++i) { const float d = v[i] - mean; q = __builtin_fmaf(d, d, q); }
    const float var = rbf(sum32(q) * (1.0f / D));
    const float m = rbf(mean), sd = rbf(__builtin_amdgcn_sqrtf(rbf(var + 1e-5f)));
    asm volatile("" ::"v"(dAr), "v"(dAi), "v"(dBr), "v"(dBi), "v"(dCr), "v"(dCi), "v"(ldtv), "v"(dskv), "v"(b1v));
    if ((lane & 31) == 0)
      __hip_atomic_store((GAS u64*)(ws + WS_ST + (tg * 8 + sw * 2 + h) * 8), ((u64)tag << 32) | pk(m, sd), RLX_AGENT);
    const float rs = 1.0f / sd;
#pragma unroll
    for (int i = 0; i < 7; ++i) {
      const u32 d = pk(v[2 * i] - m, v[2 * i + 1] - m);
      auto r = __builtin_amdgcn_permlane32_swap(__float_as_uint(lo16(d) * rs), __float_as_uint(hi16(d) * rs), false, false);
      stl<u32>(lds + LSTG + sw * 1792 + (2 * ((lane & 31) + 32 * i) + h) * 4, pk(__uint_as_float(r[0]), __uint_as_float(r[1])));
    }
    if (sw == 0) stl<u32>(lds + LTAG, tag);
  }
  post(lds, LPS, sw, 1);
  await<false>(lds, LPS, 1);
  {
    u32x4 t[2];
#pragma unroll
    for (int i = 0; i < 2; ++i) {
      LAS char* p = lds + LSTG + (sw * 112 + i * 56 + min(lane, 55)) * 4;
      t[i] = u32x4{ldl<u32>(p), ldl<u32>(p + 1792), ldl<u32>(p + 3584), ldl<u32>(p + 5376)};
    }
    post(lds, LPS, sw, 2);
    if (lane < 56) {
      stg16(rsW, lane * 16, WS_XT + (tg * D + sw * 112) * 16, t[0]);
      stg16(rsW, lane * 16, WS_XT + (tg * D + sw * 112 + 56) * 16, t[1]);
    }
  }
  STAMP(2);
  if (!mainwg) {
    WAITV(0);
    if (lane == 0) stflag(ws, WS_XF + (sw * 256 + tg) * 4, tag);
    STAMP(3);
    STAMP_FLUSH();
    EXIT();
  }
  const float dt = rbf(expf(lo16(ldtv)));
  LAS char* stg = lds + LSTG + sw * STG_W;
  f32x4 k00 = {0, 0, 0, 0}, k01 = k00, k10 = k00, k11 = k00;
  const u32 stsrc = WS_ST + (g1 * 128 + sw * 32 + (lane & 31)) * 8;
#pragma unroll 1
  for (int pass = 0; pass < 2; ++pass) {
    const int sh = pass ? 0 : 16;
    auto up = [&](u32 d) { return __uint_as_float((d << sh) & 0xffff0000u); };
    const float a2 = (dt * up(dAr)) * 1.44269504088896f, b2r = (dt * up(dAi)) * 0.15915494309189535f;
    const cpx z1 = cpow(a2, b2r, 1.f), z32 = cpow(a2, b2r, 32.f), z256 = cpow(a2, b2r, 256.f);
    const cpx cc = cmul(cpx{up(dCr), up(dCi)}, cpx{dt * up(dBr), dt * up(dBi)});
    const cpx z2 = csq(z1), z4 = csq(z2), z8 = csq(z4), z16 = csq(z8);
    LAS char* wp = stg + lane * 4;
    LAS char* rp = stg + (lane & 15) * STG_R + (lane >> 4) * 16;
    auto put = [&](int row, cpx v) { stl<u32>(wp + row * STG_R, pk(v.r, v.i)); };
    cpx b[4] = {cpx{1.f, 0.f}, z8, z16, cmul(z16, z8)};
    if (pass == 0) await<false>(lds, LPS, 2);
    FENCE();
    u32x4 fb0[4], fb1[4], fa[4];
#pragma unroll
    for (int i = 0; i < 4; ++i)
#pragma unroll
      for (int c = 0; c < 4; ++c) { put(4 * i + c, b[c]); b[c] = (i == 0 && c == 0) ? z32 : cmul(b[c], z32); }
    FENCE();
#pragma unroll
    for (int s = 0; s < 4; ++s) fb0[s] = ldl<u32x4>(rp + s * 64);
    FENCE();
#pragma unroll
    for (int i = 0; i < 4; ++i)
#pragma unroll
      for (int c = 0; c < 4; ++c) { put(4 * i + c, b[c]); if (i < 3) b[c] = cmul(b[c], z32); }
    FENCE();
#pragma unroll
    for (int s = 0; s < 4; ++s) fb1[s] = ldl<u32x4>(rp + s * 64);
    FENCE();
    u64 stg64 = 0;
    if (pass == 0) stg64 = ldflag2(ws, stsrc);
    cpx a[8];
    a[0] = cpx{cc.r, -cc.i};
    a[1] = cmulc(a[0], z1);
#pragma unroll
    for (int i = 2; i < 8; ++i) a[i] = cmulc(a[i - 2], z2);
#pragma unroll
    for (int i = 0; i < 8; ++i) put(i, a[i]);
    cpx e0 = cmulc(a[0], z256), e2 = cmulc(a[2], z256), e4 = cmulc(a[4], z256), e6 = cmulc(a[6], z256);
    put(8, e0); put(9, e2); put(10, e4); put(11, e6);
    e0 = cmulc(e0, z256); e4 = cmulc(e4, z256);
    put(12, e0); put(13, e4);
    e0 = cmulc(e0, z256); e4 = cmulc(e4, z256);
    put(14, e0); put(15, e4);
    FENCE();
#pragma unroll
    for (int s = 0; s < 4; ++s) fa[s] = ldl<u32x4>(rp + s * 64);
    FENCE();
#pragma unroll
    for (int i = 0; i < 5; ++i) { e0 = cmulc(e0, z256); put(i, e0); }
#pragma unroll
    for (int s = 0; s < 4; ++s) { k00 = mfma16(fa[s], fb0[s], k00); k01 = mfma16(fa[s], fb1[s], k01); }
    FENCE();
#pragma unroll
    for (int s = 0; s < 4; ++s) fa[s] = ldl<u32x4>(rp + s * 64);
    FENCE();
#pragma unroll
    for (int s = 0; s < 4; ++s) { k10 = mfma16(fa[s], fb0[s], k10); k11 = mfma16(fa[s], fb1[s], k11); }
    if (pass == 0) {
      WAITV(0);
      if (lane == 0) stflag(ws, WS_XF + (sw * 256 + tg) * 4, tag);
_Pragma("unroll 1")
      for (u32 n = 0; n < SPIN_CAP; ++n) {
        if (allof((u32)(stg64 >> 32) == tag)) break;
        stg64 = ldflag2(ws, stsrc);
      }
      if (lane < 32) {
        stl<u32>(lds + LSTAT + (sw * 32 + lane) * 4, (u32)stg64);
        stl<u32>(lds + LB1 + (sw * 32 + lane) * 4, b1v);
      }
      post(lds, LPS, sw, 3);
      STAMP(3);
    }
  }
  STAMP(4);
  {
    const int grp = lane >> 4, col = lane & 15;
    const u32 a0 = grp < 2 ? (8 * col + 4 * grp) * 4 : grp == 2 ? (256 + 4 * col) * 4 : (384 + 2 * col) * 4;
    const u32 d01 = grp == 3 ? 256 : 8, dbt = grp < 2 ? 512 : grp == 2 ? 256 : 128;
    stl<f32x2>(stg + a0, f32x2{k00[0], k00[1]}); stl<f32x2>(stg + a0 + d01, f32x2{k00[2], k00[3]});
    stl<f32x2>(stg + a0 + dbt, f32x2{k01[0], k01[1]}); stl<f32x2>(stg + a0 + dbt + d01, f32x2{k01[2], k01[3]});
    if (lane < 16) {
#pragma unroll
      for (int r = 0; r < 4; ++r) { stl<float>(stg + (512 + 32 * r + col) * 4, k10[r]); stl<float>(stg + (528 + 32 * r + col) * 4, k11[r]); }
    }
    if (lane == 16) stl<float>(stg + 640 * 4, k10[0]);
  }
  post(lds, LPS, sw, 4);
  {
    const u32 wfl = WS_W2F + (sw * 32 + (lane & 31)) * 4;
_Pragma("unroll 1")
    for (u32 n = 0; n < SPIN_CAP; ++n)
      if (allof((lane & 31) >= 28 || ldflag(ws, wfl) == tag)) break;
  }
  post(lds, LPS, sw, 5);
  STAMP(5);
  u32x4 uv[2];
  {
    const u32 xfl = WS_XF + ((ch / 112) * 256 + hf2 * 128 + lane) * 4;
_Pragma("unroll 1")
    for (u32 n = 0; n < SPIN_CAP; ++n) {
      const u32 f0 = ldflag(ws, xfl), f1 = ldflag(ws, xfl + 256);
      if (allof(f0 == tag && f1 == tag)) break;
    }
    FENCE();
    uv[0] = ldg16(rsW, lane * (D * 16), WS_XT + ((hf2 * 128) * D + ch) * 16);
    uv[1] = ldg16(rsW, lane * (D * 16), WS_XT + ((hf2 * 128 + 64) * D + ch) * 16);
  }
  STAMP(6);
  await<true>(lds, LPS, 4);
  await<true>(lds, LPG, 2);
  LAS char* kc = lds + LKC + cl * KC_CH;
  const int n0 = hf2 * 64 + lane, n1 = n0 + 128;
  {
    LAS char* vp = lds + LSTG + cl * 2 * STG_W;
    auto val = [&](int l) {
      const int bi = (int)(pk((float)l, 0.f)) - (0x4300 - 128);
      const int idx = l < 256 ? l : bi;
      return ldl<float>(vp + idx * 4) + ldl<float>(vp + STG_W + idx * 4);
    };
    float kv[8];
#pragma unroll
    for (int p = 0; p < 8; ++p) kv[p] = val(8 * n0 + p);
    if (n0 == 0) kv[0] += lo16(dskv);
    stl<u32x4>(kc + (n0 + 1) * 16, u32x4{pk(kv[7], kv[6]), pk(kv[5], kv[4]), pk(kv[3], kv[2]), pk(kv[1], kv[0])});
    LAS char* v3 = vp + (512 + n0) * 4;
    const float va = ldl<float>(v3) + ldl<float>(v3 + STG_W), vc = ldl<float>(v3 + 4) + ldl<float>(v3 + STG_W + 4);
    const u32 aa = pk(va, va);
    stl<u32x4>(kc + (n1 + 1) * 16, u32x4{pk(vc, vc), pk(vc, (lane & 1) ? vc : va), aa, aa});
    if (hf2 == 0 && lane == 0) stl<u32x4>(kc, u32x4{0u, 0u, 0u, 0u});
  }
  post(lds, LPS, sw, 6);
  await<false>(lds, LPS, 6);
  STAMP(7);
#pragma unroll
  for (int it = 0; it < 2; ++it) {
    LAS char* p = kc + (it ? n1 : n0) * 16;
    const u32x4 cu = ldl<u32x4>(p + 16), pv = ldl<u32x4>(p);
    const u32 d[8] = {cu[0], cu[1], cu[2], cu[3], pv[0], pv[1], pv[2], pv[3]};
    u32 o[7];
#pragma unroll
    for (int i = 0; i < 7; ++i) o[i] = __builtin_amdgcn_alignbit(d[i + 1], d[i], 16);
#pragma unroll
    for (int s = 1; s < 8; ++s) {
      const int m = s >> 1;
      stl<u32x4>(p + 16 + s * KC_CP, (s & 1) ? u32x4{o[m], o[m + 1], o[m + 2], o[m + 3]} : u32x4{d[m], d[m + 1], d[m + 2], d[m + 3]});
    }
  }
  LAS char* ub = lds + LU + cl * U_CH;
  if (hf2 == 0) {
    if (lane < 8) stl<u32x4>(kc + lane * KC_CP, u32x4{0u, 0u, 0u, 0u});
    if (lane < 32) stl<u32x4>(ub + lane * 16, u32x4{0u, 0u, 0u, 0u});
  }
  stl<u32x4>(ub + 512 + (hf2 * 128 + lane) * 16, uv[0]);
  stl<u32x4>(ub + 512 + (hf2 * 128 + 64 + lane) * 16, uv[1]);
  post(lds, LPS, sw, 7);
  await<false>(lds, LPS, 7);
  STAMP(8);
  if (blockIdx.x == 0 && sw == 0 && lane == 0) stflag(ws, WS_EPOCH, tag);
  f32x4 y[8];
#pragma unroll
  for (int q = 0; q < 8; ++q) y[q] = f32x4{0, 0, 0, 0};
  {
    const int kq = lane >> 4;
    LAS char* ka = kc + (7 - (lane & 7)) * KC_CP + (16 * hf2 + 3 + ((lane >> 3) & 1) - kq) * 16;
    LAS char* ua = ub + 512 + (16 * (lane & 15) - 128 * hf2 - 16 + 8 * kq) * 2;
#pragma unroll
    for (int m = 0; m < 4; ++m) {
      u32x4 A[8], B[8];
#pragma unroll
      for (int p = 0; p < 8; ++p) { A[p] = ldl<u32x4>(ka + m * 64 + p * 512); B[p] = ldl<u32x4>(ua - m * 64 + p * 512); }
#pragma unroll
      for (int p = 0; p < 8; ++p)
#pragma unroll
        for (int q = 0; q + p < 8; ++q) y[p + q] = mfma16(A[p], B[q], y[p + q]);
    }
  }
  STAMP(9);
  await<true>(lds, LPG, 5);
#pragma unroll
  for (int q = 0; q < 8; ++q) stl<f32x4>(lds + LY + sw * 8192 + (256 * q + 16 * (lane & 15) + 4 * (lane >> 4)) * 4, y[q]);
  post(lds, LPS, sw, 8);
  await<false>(lds, LPS, 8);
  {
    u32x4 o[2];
#pragma unroll
    for (int it = 0; it < 2; ++it) {
      LAS char* p = lds + LY + cl * 16384 + (hf2 * 1024 + it * 512 + lane * 8) * 4;
      const f32x4 a = ldl<f32x4>(p) + ldl<f32x4>(p + 8192), b = ldl<f32x4>(p + 16) + ldl<f32x4>(p + 8192 + 16);
      o[it] = u32x4{pk(a[0], a[1]), pk(a[2], a[3]), pk(b[0], b[1]), pk(b[2], b[3])};
    }
    post(lds, LPS, sw, 9);
    stg16(rsW, lane * 16, WS_YA + (ch * L + hf2 * 1024) * 2, o[0]);
    stg16(rsW, lane * 16, WS_YA + (ch * L + hf2 * 1024 + 512) * 2, o[1]);
  }
  STAMP(10);
  WAITV(0);
  if (lane == 0) stflag(ws, WS_YF + (hf2 * 512 + ch) * 4, tag);
  STAMP(11);
  const int tok0 = g2 * 128 + th * 64 + sw * 16, oc = lane & 7;
  float bb[2][8], s2[2];
  {
    u32x4 xv[2];
    u64 sv[2];
#pragma unroll
    for (int it = 0; it < 2; ++it) xv[it] = *(const u32x4*)(x + (tok0 + it * 8 + (lane >> 3)) * D + jc * 64 + oc * 8);
    const u32x4 bv = *(const u32x4*)(b2 + jc * 64 + oc * 8);
    const u32 yfl = WS_YF + ((g2 >> 3) * 512 + jc * 64 + lane) * 4, so = WS_ST + (tok0 + (lane >> 3)) * 8;
_Pragma("unroll 1")
    for (u32 n = 0; n < SPIN_CAP; ++n) {
      const u32 f = ldflag(ws, yfl);
      sv[0] = ldflag2(ws, so); sv[1] = ldflag2(ws, so + 64);
      if (allof(f == tag && (u32)(sv[0] >> 32) == tag && (u32)(sv[1] >> 32) == tag)) break;
      __builtin_amdgcn_s_sleep(2);
    }
    FENCE();
    STAMP(12);
    LAS char* yt = lds + sw * 2048;
#pragma unroll
    for (int i = 0; i < 2; ++i) {
      const u32x4 t = ldg16(rsW, (lane >> 1) * (L * 2) + (lane & 1) * 16, WS_YA + ((jc * 64 + i * 32) * L + tok0) * 2);
      stl<u32x4>(yt + i * 1024 + lane * 16, t);
    }
    FENCE();
#pragma unroll
    for (int it = 0; it < 2; ++it) {
      const float sd = hi16((u32)sv[it]);
      s2[it] = 1.0f / (sd * sd);
#pragma unroll
      for (int e = 0; e < 8; ++e) {
        const float ya = lo16(ldl<u16>(yt + (oc * 8 + e) * 32 + (it * 8 + (lane >> 3)) * 2));
        const float xe = (e & 1) ? hi16(xv[it][e >> 1]) : lo16(xv[it][e >> 1]);
        const float be = (e & 1) ? hi16(bv[e >> 1]) : lo16(bv[e >> 1]);
        bb[it][e] = (xe + ya) + be;
      }
    }
  }
  STAMP(13);
  await<false>(lds, LPG, 6);
  STAMP(14);
#pragma unroll
  for (int it = 0; it < 2; ++it) {
    LAS char* p = lds + LP + (sw * 16 + it * 8 + (lane >> 3)) * P_R + oc * 32;
    const f32x4 a = (ldl<f32x4>(p) + ldl<f32x4>(p + P_W)) + (ldl<f32x4>(p + 2 * P_W) + ldl<f32x4>(p + 3 * P_W));
    const f32x4 b = (ldl<f32x4>(p + 16) + ldl<f32x4>(p + P_W + 16)) + (ldl<f32x4>(p + 2 * P_W + 16) + ldl<f32x4>(p + 3 * P_W + 16));
    u32x4 o;
#pragma unroll
    for (int e = 0; e < 2; ++e) {
      o[e] = pk(__builtin_fmaf(a[2 * e], s2[it], bb[it][2 * e]), __builtin_fmaf(a[2 * e + 1], s2[it], bb[it][2 * e + 1]));
      o[2 + e] = pk(__builtin_fmaf(b[2 * e], s2[it], bb[it][4 + 2 * e]), __builtin_fmaf(b[2 * e + 1], s2[it], bb[it][5 + 2 * e]));
    }
    *(u32x4*)(out + (tok0 + it * 8 + (lane >> 3)) * D + jc * 64 + oc * 8) = o;
  }
  STAMP(15);

  STAMP_FLUSH();
  EXIT();
  }
  int wv = wave;
  asm volatile("" : "+s"(wv));
  {
    if (!mainwg) {
      if (ks < 112) {
        const int sg = ks / 56, jj = (ks % 56) >> 2;
        WAITV(0);
        STAMP(1);
        LAS char* a = lds + xs + (4 * h + ((lane & 15) >> 2)) * 896 + ((lane >> 4) & 1) * 32 + (lane & 3) * 8;
#pragma unroll
        for (int t = 0; t < 14; ++t) {
          u32x4 f = trfrag(a + t * 64, 8 * 896);
          stg16(rsW, lane * 16, WS_W2T + (((((t >> 1) * 2 + sg) * 4 + wv) * 14 + jj) * 2 + (t & 1)) * 1024, f);
        }
        await<true>(lds, LPS, 1);
        const u32 tag = ldl<u32>(lds + LTAG);
        STAMP(2);
        WAITV(0);
        if (lane == 0) stflag(ws, WS_W2F + (wv * 32 + sg * 14 + jj) * 4, tag);
        STAMP(3);
      }
      STAMP_FLUSH();
      EXIT();
    }
    STAMP(1);
    const int q4 = (lane & 15) >> 2;
    const u32 aW = (8 * h + q4) * 128 + ((((lane >> 4) & 1) * 32 + (lane & 3) * 8) ^ ((q4 >> 1) * 64));
    const u32 aX = (lane & 31) * 128 + 16 * (h ^ ((lane >> 1) & 7));
    const u32 selw = ((lane & 15) == ((lane >> 4) & 1)) ? 0x3F803F80u : 0u;
    const u32x4 sel = {selw, selw, selw, selw};
    WAITV(20);
    STAMP(2);
    {
      f32x4 c0 = {0, 0, 0, 0}, c1 = {0, 0, 0, 0};
#pragma unroll
      for (int s = 0; s < 7; ++s) {
        LAS char* p = lds + LW1A + (wv * 7 + s) * 2048;
        c0 = mfma16(sel, trfrag(p + aW, 512), c0);
        c1 = mfma16(sel, trfrag(p + (aW ^ 64), 512), c1);
      }
      if (lane < 16) {
        LAS char* p = lds + LCS + wv * 256 + lane * 4;
        stl<float>(p, c0[0]); stl<float>(p + 64, c0[1]); stl<float>(p + 128, c1[0]); stl<float>(p + 192, c1[1]);
      }
    }
    post(lds, LPG, wv, 1);
    await<false>(lds, LPG, 1);
    STAMP(3);
    u32x4 xf[28];
    f32x16 acc0, acc1;
#pragma unroll
    for (int i = 0; i < 16; ++i) { acc0[i] = 0.f; acc1[i] = 0.f; }
    u32x4 af[2][8];
#pragma unroll
    for (int s = 0; s < 4; ++s) { af[0][2 * s] = trfrag(lds + LW1A + s * 2048 + aW, 512); af[0][2 * s + 1] = trfrag(lds + LW1A + s * 2048 + (aW ^ 64), 512); }
#pragma unroll
    for (int c = 0; c < 7; ++c) {
      if (c < 6) WAITV(16); else WAITV(14);
      const u32 slot = xs + (c % 5) * 4096;
#pragma unroll
      for (int s = 0; s < 4; ++s) xf[4 * c + s] = ldl<u32x4>(lds + slot + (aX ^ (32 * s)));
      if (c < 6) {
#pragma unroll
        for (int s = 0; s < 4; ++s) {
          LAS char* p = lds + LW1A + (4 * c + 4 + s) * 2048;
          af[(c + 1) & 1][2 * s] = trfrag(p + aW, 512); af[(c + 1) & 1][2 * s + 1] = trfrag(p + (aW ^ 64), 512);
        }
      }
      asm volatile("" : "+v"(xf[4 * c]), "+v"(xf[4 * c + 1]), "+v"(xf[4 * c + 2]), "+v"(xf[4 * c + 3]));
      if (c < 2) {
#pragma unroll
        for (int i = 0; i < 4; ++i) dma(lb + slot + i * 1024, (i & 1) ? vX1 : vX0, rsx, sX + i * 8 * 896 + (c + 5) * 128);
      } else if (c < 6) {
#pragma unroll
        for (int i = 0; i < (c < 5 ? 4 : 2); ++i) {
          const int n = 2 * (c - 2) + (i >> 1);
          dma(lb + slot + i * 1024, vW, rs1, ((4 * n + wv) * 16 + (i & 1) * 8) * 3584 + 1792 + j1 * 128);
        }
      }
#pragma unroll
      for (int s = 0; s < 4; ++s) {
        acc0 = mfma32(af[c & 1][2 * s], xf[4 * c + s], acc0);
        acc1 = mfma32(af[c & 1][2 * s + 1], xf[4 * c + s], acc1);
      }
      SCHED();
    }
    post(lds, LPG, wv, 2);
    STAMP(4);
    await<false>(lds, LPS, 3);
    STAMP(5);
    const u32 tag = __builtin_amdgcn_readfirstlane(ldl<u32>(lds + LTAG));
    u32x4 bg0, bg1;
    {
      const u32 st = ldl<u32>(lds + LSTAT + (wv * 32 + (lane & 31)) * 4);
      const u32 nm = (st & 0xffffu) ^ 0x8000u;
      const u32 d0 = nm | (nm << 16), d1 = st >> 16;
      bg0 = u32x4{h ? 0u : d0, h ? 0u : d1, 0u, 0u};
      bg1 = u32x4{h ? d0 : 0u, h ? d1 : 0u, 0u, 0u};
    }
    auto aug = [&](int sg) {
      LAS char* p = lds + LCS + sg * 1024 + lane * 4;
      const float cs = (ldl<float>(p) + ldl<float>(p + 256)) + (ldl<float>(p + 512) + ldl<float>(p + 768));
      const float hi = rbf(cs);
      const u32x4 a = {pk(hi, cs - hi), ldl<u32>(lds + LB1 + sg * 256 + lane * 4), 0u, 0u};
      acc0 = mfma32(a, bg0, acc0);
      acc1 = mfma32(a, bg1, acc1);
    };
    const u32 hdst = WS_H + ((g1 * 2 + (wv >> 1)) * 8 * 14 + j1) * 2048 + (wv & 1) * 1024;
    auto epi = [&](int sg) {
#pragma unroll
      for (int kw = 0; kw < 4; ++kw) {
        u32x4 o;
#pragma unroll
        for (int e = 0; e < 4; ++e) {
          const int i = 8 * (kw & 1) + 2 * e;
          float a = (kw >> 1) ? acc1[i] : acc0[i], b = (kw >> 1) ? acc1[i + 1] : acc0[i + 1];
          a = fmaxf(a, 0.f); b = fmaxf(b, 0.f);
          o[e] = pk(a * a, b * b);
        }
        stg16(rsW, lane * 16, hdst + (sg * 4 + kw) * 14 * 2048, o);
      }
    };
    auto bptr = [&](int s) -> LAS char* { const int n = s >> 2; return lds + LXS + (s & 3) * XS_W + (n < 6 ? 8192 + 2048 * n : 0); };
    aug(0);
    WAITV(6);
    post(lds, LPG, wv, 3);
    f32x4 c0 = {0, 0, 0, 0}, c1 = {0, 0, 0, 0};
#pragma unroll
    for (int n = 0; n < 4; ++n) {
      LAS char* p = lds + xs + 8192 + 2048 * n;
      c0 = mfma16(sel, trfrag(p + aW, 512), c0);
      c1 = mfma16(sel, trfrag(p + (aW ^ 64), 512), c1);
    }
    epi(0);
    STAMP(6);
    await<false>(lds, LPG, 3);
    STAMP(7);
#pragma unroll
    for (int i = 0; i < 16; ++i) { acc0[i] = 0.f; acc1[i] = 0.f; }
    u32x4 wf[28][2];
    const u32 wsrc = WS_W2T + (jc * 8 + wv) * 14 * 2048;
#pragma unroll
    for (int s = 0; s < 4; ++s) { af[0][2 * s] = trfrag(bptr(s) + aW, 512); af[0][2 * s + 1] = trfrag(bptr(s) + (aW ^ 64), 512); }
#pragma unroll
    for (int c = 0; c < 7; ++c) {
      if (c == 2) {
        WAITV(4);
#pragma unroll
        for (int n = 4; n < 7; ++n) {
          LAS char* p = lds + xs + (n < 6 ? 8192 + 2048 * n : 0);
          c0 = mfma16(sel, trfrag(p + aW, 512), c0);
          c1 = mfma16(sel, trfrag(p + (aW ^ 64), 512), c1);
        }
        if (lane < 16) {
          LAS char* p = lds + LCS + 1024 + wv * 256 + lane * 4;
          stl<float>(p, c0[0]); stl<float>(p + 64, c0[1]); stl<float>(p + 128, c1[0]); stl<float>(p + 192, c1[1]);
        }
        post(lds, LPG, wv, 4);
      }
      if (c == 3) await<false>(lds, LPG, 4);
      if (c < 6) {
#pragma unroll
        for (int s = 0; s < 4; ++s) {
          LAS char* p = bptr(4 * c + 4 + s);
          af[(c + 1) & 1][2 * s] = trfrag(p + aW, 512); af[(c + 1) & 1][2 * s + 1] = trfrag(p + (aW ^ 64), 512);
        }
      }
      if (c == 5) {
        await<false>(lds, LPS, 5);
#pragma unroll
        for (int i = 0; i < 8; ++i) { wf[i][0] = ldg16(rsW, lane * 16, wsrc + i * 2048); wf[i][1] = ldg16(rsW, lane * 16, wsrc + i * 2048 + 1024); }
      }
      if (c == 6) {
        WAITV(16);
        if (lane == 0) stflag(ws, WS_HF + (((g1 * 2 + (wv >> 1)) * 2 + 0) * 32 + (wv & 1) * 16 + j1) * 4, tag);
      }
#pragma unroll
      for (int s = 0; s < 4; ++s) {
        acc0 = mfma32(af[c & 1][2 * s], xf[4 * c + s], acc0);
        acc1 = mfma32(af[c & 1][2 * s + 1], xf[4 * c + s], acc1);
      }
      SCHED();
    }
    post(lds, LPG, wv, 5);
    aug(1);
    STAMP(8);
    epi(1);
    SCHED();
#pragma unroll
    for (int i = 8; i < 14; ++i) { wf[i][0] = ldg16(rsW, lane * 16, wsrc + i * 2048); wf[i][1] = ldg16(rsW, lane * 16, wsrc + i * 2048 + 1024); }
    STAMP(9);
    WAITV(12);
    if (lane == 0) stflag(ws, WS_HF + (((g1 * 2 + (wv >> 1)) * 2 + 1) * 32 + (wv & 1) * 16 + j1) * 4, tag);
    STAMP(10);
    const u32 hfl = WS_HF + ((g2 * 2 + th) * 2) * 128 + (lane & 31) * 4;
    const bool hskip = (lane & 15) >= 14;
_Pragma("unroll 1")
    for (u32 n = 0; n < SPIN_CAP; ++n)
      if (allof(hskip || ldflag(ws, hfl) == tag)) break;
    FENCE();
    STAMP(11);
    const u32 hsrc = WS_H + ((g2 * 2 + th) * 8 + wv) * 14 * 2048;
    u32x4 hf[28][2];
    auto ldh = [&](int i) { const u32 o = hsrc + (i / 14) * 4 * 14 * 2048 + (i % 14) * 2048; hf[i][0] = ldg16(rsW, lane * 16, o); hf[i][1] = ldg16(rsW, lane * 16, o + 1024); };
    auto ldw = [&](int i) { const u32 o = wsrc + (i / 14) * 4 * 14 * 2048 + (i % 14) * 2048; wf[i][0] = ldg16(rsW, lane * 16, o); wf[i][1] = ldg16(rsW, lane * 16, o + 1024); };
    f32x16 o00, o01, o10, o11;
#pragma unroll
    for (int i = 0; i < 16; ++i) { o00[i] = 0.f; o01[i] = 0.f; o10[i] = 0.f; o11[i] = 0.f; }
#pragma unroll
    for (int i = 0; i < 4; ++i) ldh(i);
    SCHED();
    u32 fb = 0;
#pragma unroll
    for (int i = 0; i < 28; ++i) {
      o00 = mfma32(wf[i][0], hf[i][0], o00);
      o01 = mfma32(wf[i][0], hf[i][1], o01);
      o10 = mfma32(wf[i][1], hf[i][0], o10);
      o11 = mfma32(wf[i][1], hf[i][1], o11);
      SCHED();
      if (i < 5) { ldh(4 + 2 * i); ldh(5 + 2 * i); }
      if (i == 4) fb = ldflag(ws, hfl + 128);
      if (i >= 5 && i < 8) { ldw(14 + 2 * (i - 5)); ldw(15 + 2 * (i - 5)); }
      if (i == 8) {
        if (!allof(hskip || fb == tag))
_Pragma("unroll 1")
          for (u32 n = 0; n < SPIN_CAP; ++n)
            if (allof(hskip || ldflag(ws, hfl + 128) == tag)) break;
        FENCE();
        STAMP(12);
      }
      if (i >= 8 && i < 11) { ldh(14 + 2 * (i - 8)); ldh(15 + 2 * (i - 8)); }
      if (i >= 11 && i < 19) { ldw(20 + (i - 11)); ldh(20 + (i - 11)); }
      SCHED();
    }
    STAMP(13);
    await<false>(lds, LPS, 9);
    {
      LAS char* p = lds + LP + wv * P_W + (lane & 31) * P_R + h * 16;
#pragma unroll
      for (int qd = 0; qd < 4; ++qd) {
        stl<f32x4>(p + qd * 32, f32x4{o00[4 * qd], o00[4 * qd + 1], o00[4 * qd + 2], o00[4 * qd + 3]});
        stl<f32x4>(p + qd * 32 + 32 * P_R, f32x4{o01[4 * qd], o01[4 * qd + 1], o01[4 * qd + 2], o01[4 * qd + 3]});
        stl<f32x4>(p + qd * 32 + 128, f32x4{o10[4 * qd], o10[4 * qd + 1], o10[4 * qd + 2], o10[4 * qd + 3]});
        stl<f32x4>(p + qd * 32 + 128 + 32 * P_R, f32x4{o11[4 * qd], o11[4 * qd + 1], o11[4 * qd + 2], o11[4 * qd + 3]});
      }
    }
    post(lds, LPG, wv, 6);
    STAMP(14);
    STAMP_FLUSH();
    EXIT();
  }

}

__global__ void empty_small(char* ws) { if (ws == nullptr) *(volatile int*)ws = 0; }
__global__ __launch_bounds__(512, 2) void empty_big(char* ws) {
  __shared__ __attribute__((aligned(16))) char sm[LDS_BYTES];
  if (ws == nullptr) { asm volatile("v_mov_b32 v247, 0" ::: "v247"); *(volatile char*)(sm + threadIdx.x) = 0; }
}
}

extern "C" void kernel_launch(void* const* d_in, const int* in_sizes, int n_in, void* d_out, int out_size, void* d_ws,
                              size_t ws_size, hipStream_t stream) {
  fused_kernel<<<256, 512, 0, stream>>>((const u16*)d_in[0], (const u16*)d_in[1], (const u16*)d_in[2], (const u16*)d_in[3],
                                        (const u16*)d_in[4], (const u16*)d_in[5], (const u16*)d_in[6], (const u16*)d_in[7],
                                        (const u16*)d_in[8], (const u16*)d_in[10], (const u16*)d_in[11], (const u16*)d_in[12],
                                        (const u16*)d_in[13], (u16*)d_out, (char*)d_ws);
  empty_small<<<1, 64, 0, stream>>>((char*)d_ws);
}
```

```cpp
#include <hip/hip_runtime.h>
#include <stdint.h>
#include <type_traits>

namespace {

typedef unsigned short u16;
typedef unsigned u32;
typedef unsigned long long u64;
#define LAS __attribute__((address_space(3)))
#define GAS __attribute__((address_space(1)))
#define DI __device__ __forceinline__
typedef __attribute__((ext_vector_type(2))) u32 u32x2;
typedef __attribute__((ext_vector_type(4))) u32 u32x4;
typedef __attribute__((ext_vector_type(2))) float f32x2;
typedef __attribute__((ext_vector_type(4))) float f32x4;
typedef __attribute__((ext_vector_type(4))) short s16x4;
typedef __attribute__((ext_vector_type(2))) __bf16 bf16x2;
typedef __attribute__((ext_vector_type(8))) __bf16 bf16x8;
typedef __attribute__((ext_vector_type(2))) _Float16 f16x2;
typedef __attribute__((ext_vector_type(8))) _Float16 f16x8;
typedef __amdgpu_buffer_rsrc_t rsrc_t;

constexpr int L = 2048, D = 448, F = 1792;

constexpr u32 WS_EPOCH = 0, WS_ST = 4096, WS_CS = 20480;
constexpr u32 WS_TF = 36864, WS_WF = 40960, WS_XF = 65536, WS_HF = 1u << 17, WS_YF = 1u << 18;
constexpr u32 WS_XT = 1u << 20, WS_YA = 3u << 20, WS_W2T = 5u << 20, WS_H = 8u << 20, WS_END = 16u << 20;
constexpr u32 XSTR = 7168, YSTR = 4352, TSTR = 17664;
constexpr u32 TAG_MUL = 1664525u, TAG_ADD = 1013904223u;
constexpr u32 OOB = 0x40000000u;

constexpr u32 LSTG = 0, STG_W = 4608, STG_R = 288, LU = 18432, U_CH = 4608, LPG = 27648, LPS = 27652, LTAG = 27656;
constexpr u32 LKC = 32256, KC_CH = 16640, KC_CP = 4160, SLOT = 32768;
constexpr u32 LYT = LKC, YT_O = 1040, LCW = 65536, LCP = 28672;
constexpr u32 sbase(int s) { return ((s + 2) % 4 + 1) * SLOT; }
constexpr u32 LDS_BYTES = 163840;
constexpr u32 SPIN_CAP = 1u << 18;

#define FENCE() asm volatile("" ::: "memory")
#define WAITV(n) asm volatile("s_waitcnt vmcnt(%0)" ::"i"(n) : "memory")
#define EXIT() do { asm volatile("s_endpgm" ::: "memory"); __builtin_amdgcn_s_waitcnt(0); } while (0)
#define DIE() do { EXIT(); asm volatile("" : "=v"(xa), "=v"(xb), "=v"(eps), "=v"(dAr), "=v"(dAi), "=v"(dBr), "=v"(dBi), "=v"(dCr), "=v"(dCi), "=v"(pfd)); asm volatile("" : "=s"(ldt2), "=s"(dsk2)); } while (0)
#define LANES() int lane; asm volatile("v_mbcnt_lo_u32_b32 %0, -1, 0\n\tv_mbcnt_hi_u32_b32 %0, -1, %0" : "=v"(lane))
#define HI() do { SCHED(); __builtin_amdgcn_s_setprio(2); SCHED(); } while (0)
#define LO() do { SCHED(); __builtin_amdgcn_s_setprio(0); SCHED(); } while (0)
#define BAR() do { SCHED(); __builtin_amdgcn_s_barrier(); FENCE(); SCHED(); } while (0)
#define SCHED() __builtin_amdgcn_sched_barrier(0)
#define RSRC(p, n) __builtin_amdgcn_make_buffer_rsrc((void*)(p), 0, n, 0x00020000)
#define RLX_AGENT __ATOMIC_RELAXED, __HIP_MEMORY_SCOPE_AGENT

DI float lo16(u32 d) { return __uint_as_float(d << 16); }
DI float hi16(u32 d) { return __uint_as_float(d & 0xffff0000u); }
DI u32 pk(float a, float b) { f32x2 v = {a, b}; return __builtin_bit_cast(u32, __builtin_convertvector(v, bf16x2)); }
DI float rbf(float a) { return lo16(pk(a, a)); }
DI f32x4 mfma16(u32x4 a, u32x4 b, f32x4 c) { return __builtin_amdgcn_mfma_f32_16x16x32_bf16(__builtin_bit_cast(bf16x8, a), __builtin_bit_cast(bf16x8, b), c, 0, 0, 0); }
DI f32x4 mfma16h(u32x4 a, u32x4 b, f32x4 c) { return __builtin_amdgcn_mfma_f32_16x16x32_f16(__builtin_bit_cast(f16x8, a), __builtin_bit_cast(f16x8, b), c, 0, 0, 0); }
template <class T> DI T ldl(LAS char* p) { return *(LAS T*)p; }
template <class T> DI void stl(LAS char* p, T v) { *(LAS T*)p = v; }
DI u32x4 trfrag(LAS char* p, u32 step) {
  u32x2 a = __builtin_bit_cast(u32x2, __builtin_amdgcn_ds_read_tr16_b64_v4i16((LAS s16x4*)p));
  u32x2 b = __builtin_bit_cast(u32x2, __builtin_amdgcn_ds_read_tr16_b64_v4i16((LAS s16x4*)(p + step)));
  return u32x4{a[0], a[1], b[0], b[1]};
}
DI void dma(u32 ldsaddr, u32 voff, rsrc_t rs, u32 soff) {
  asm volatile("s_mov_b32 m0, %0\n\ts_nop 0\n\tbuffer_load_dwordx4 %1, %2, %3 offen lds" ::"s"(ldsaddr), "v"(voff), "s"(rs), "s"(soff) : "memory");
}
template <u32 LO, u32 SO> DI void dmai(u32 lbase, u32 voff, rsrc_t rs, u32 sbase) {
  u32 t;
  asm volatile("s_add_u32 m0, %1, %5\n\ts_add_u32 %0, %4, %6\n\tbuffer_load_dwordx4 %2, %3, %0 offen lds"
               : "=&s"(t) : "s"(lbase), "v"(voff), "s"(rs), "s"(sbase), "i"(LO), "i"(SO) : "scc", "memory");
}
template <u32 LO> DI void dma0(u32 lbase, u32 voff, rsrc_t rs, u32 soff) {
  asm volatile("s_add_u32 m0, %0, %4\n\ts_nop 0\n\tbuffer_load_dwordx4 %1, %2, %3 offen lds" ::"s"(lbase), "v"(voff), "s"(rs), "s"(soff), "i"(LO) : "scc", "memory");
}
template <u32 IO> DI void dman_(u32 voff, rsrc_t rs, u32 soff) {
  asm volatile("buffer_load_dwordx4 %0, %1, %2 offen offset:%3 lds" ::"v"(voff), "s"(rs), "s"(soff), "i"(IO) : "memory");
}
template <int N, class Fn> DI void sfor(Fn&& f) {
  if constexpr (N > 0) { sfor<N - 1>(f); f(std::integral_constant<int, N - 1>{}); }
}
DI u32x4 ldg16(rsrc_t rs, u32 voff, u32 soff) { return __builtin_amdgcn_raw_buffer_load_b128(rs, voff, soff, 16); }
DI void stg16(rsrc_t rs, u32 voff, u32 soff, u32x4 v) { __builtin_amdgcn_raw_buffer_store_b128(v, rs, voff + soff, 0, 16); }
DI u32 ldflag(char* ws, u32 off) { return __hip_atomic_load((GAS u32*)(ws + off), RLX_AGENT); }
DI void stflag(char* ws, u32 off, u32 v) { __hip_atomic_store((GAS u32*)(ws + off), v, RLX_AGENT); }
DI bool allof(bool p) { return __builtin_amdgcn_ballot_w64(!p) == 0; }

DI void post(LAS char* lds, u32 base, int w, u32 v) { FENCE(); *(volatile LAS unsigned char*)(lds + base + w) = 0x80u | v; FENCE(); }
DI u32 peek(LAS char* lds, u32 base) { return *(volatile LAS u32*)(lds + base); }
template <u32 V> DI void await(LAS char* lds, u32 base, u32 pv, u32 mask) {
  u32 t, n;
  asm volatile(
      "s_mov_b32 %2, 0x800000\n"
      "1:\n\tv_readfirstlane_b32 %1, %0\n\ts_sub_u32 %1, %1, %4\n\ts_andn2_b32 %1, %5, %1\n\ts_cbranch_scc0 2f\n\t"
      "ds_read_b32 %0, %3\n\ts_sub_u32 %2, %2, 1\n\ts_waitcnt lgkmcnt(0)\n\ts_cbranch_scc0 1b\n2:"
      : "+v"(pv), "=&s"(t), "=&s"(n) : "v"((u32)(size_t)(lds + base)), "i"(V * 0x01010101u), "s"(mask) : "scc", "memory");
}
template <u32 V> DI void await(LAS char* lds, u32 base, u32 mask = 0x80808080u) { FENCE(); await<V>(lds, base, peek(lds, base), mask); }
template <int CTRL> DI float dppadd(float v) { return v + __int_as_float(__builtin_amdgcn_update_dpp(0, __float_as_int(v), CTRL, 0xf, 0xf, true)); }
DI float dot2(u32 a, u32 b, float c) { return __builtin_amdgcn_fdot2_f32_bf16(__builtin_bit_cast(bf16x2, a), __builtin_bit_cast(bf16x2, b), c, false); }
DI float addswap32(float a, float b) { auto r = __builtin_amdgcn_permlane32_swap(__float_as_uint(a), __float_as_uint(b), false, false); return __uint_as_float(r[0]) + __uint_as_float(r[1]); }
DI float addswap16(float a, float b) { auto r = __builtin_amdgcn_permlane16_swap(__float_as_uint(a), __float_as_uint(b), false, false); return __uint_as_float(r[0]) + __uint_as_float(r[1]); }
constexpr int wslot(int v) { return v % 3 == 0 ? 3 : v % 3 - 1; }
constexpr int gap_read(int n) {
  constexpr int t[32] = {0, 8, 9, 10, 11, 1, -1, -1, 2, 12, -1, 3, 13, 14, -1, 15, 4, -1, 5, -1, -1, 6, -1, -1, 7, -1, -1, -1, -1, -1, -1, -1};
  return t[n];
}
constexpr int gap_dma(int n, bool extra) {
  constexpr unsigned base = 1u << 6 | 1u << 10 | 1u << 14 | 1u << 17 | 1u << 20 | 1u << 23 | 1u << 26 | 1u << 29;
  const unsigned m = extra ? base | 1u << 7 | 1u << 19 | 1u << 22 | 1u << 27 : base;
  return (m >> n & 1) ? __builtin_popcount(m & ((1u << n) - 1)) : -1;
}
constexpr int hord(int n) { return n < 44 ? n : 44 + ((n - 44) % 6 >> 1) * 4 + (n - 44) / 6 * 2 + (n & 1); }
struct cpx { float r, i; };
DI cpx cmul(cpx a, cpx b) { return {__builtin_fmaf(a.r, b.r, -(a.i * b.i)), __builtin_fmaf(a.r, b.i, a.i * b.r)}; }
DI cpx cpow(float a2, float b2, float k) {
  float e = __builtin_amdgcn_exp2f(a2 * k), f = __builtin_amdgcn_fractf(b2 * k);
  return {e * __builtin_amdgcn_cosf(f), e * __builtin_amdgcn_sinf(f)};
}
DI u32 pkh(cpx v) { f32x2 t = {v.r, v.i}; return __builtin_bit_cast(u32, __builtin_convertvector(t, f16x2)); }
template <bool CONJ, int N> DI void hprod(u32 (&rw)[2][53], int o, const int (&a)[N], const u32 (&q)[2]) {
  f16x2 t[2][N];
#pragma unroll
  for (int i = 0; i < N; ++i)
#pragma unroll
    for (int p = 0; p < 2; ++p) t[p][i] = __builtin_bit_cast(f16x2, rw[p][a[i]]) * __builtin_bit_cast(f16x2, q[p]).xx;
  SCHED();
#pragma unroll
  for (int i = 0; i < N; ++i)
#pragma unroll
    for (int p = 0; p < 2; ++p) {
      const f16x2 v = __builtin_bit_cast(f16x2, rw[p][a[i]]);
      rw[p][o + i] = __builtin_bit_cast(u32, __builtin_elementwise_fma(CONJ ? f16x2{v.y, -v.x} : f16x2{-v.y, v.x}, __builtin_bit_cast(f16x2, q[p]).yy, t[p][i]));
    }
#pragma unroll
  for (int i = 0; i < N; ++i) asm volatile("" : "+v"(rw[0][o + i]), "+v"(rw[1][o + i]));
  SCHED();
}
template <int CTRL> DI float dppmax(float v) { return fmaxf(v, __int_as_float(__builtin_amdgcn_update_dpp(0, __float_as_int(v), CTRL, 0xf, 0xf, true))); }

__global__ __launch_bounds__(512, 2) void fused_kernel(
    const u16* x, const u16* w1, char* ws, const u16* __restrict__ pAr, const u16* __restrict__ pAi,
    const u16* __restrict__ pBr, const u16* __restrict__ pBi, const u16* __restrict__ pCr, const u16* __restrict__ pCi,
    const u16* __restrict__ pdt, const u16* __restrict__ pds, const u16* __restrict__ b1, const u16* __restrict__ w2,
    const u16* __restrict__ b2, u16* __restrict__ out) {
  __shared__ __attribute__((aligned(16))) char smem[LDS_BYTES];
  LAS char* lds = (LAS char*)smem;
  const u32 lb = (u32)(size_t)lds;
  const int lane = threadIdx.x & 63, wave = __builtin_amdgcn_readfirstlane(threadIdx.x >> 6);
  const int xcd = blockIdx.x, jc = blockIdx.y, zq = blockIdx.z;
  const bool mainwg = jc < 7;
  u32 pfd;
  {
    u64 pc;
    u32 o = ((zq * 8 + jc) * 8 + wave) * 128;
    asm volatile("s_getpc_b64 %0\n.Lcode_here:\n\ts_min_u32 %1, %1, .Lcode_last-.Lcode_here" : "=s"(pc), "+s"(o)::"scc");
    asm volatile("global_load_dword %0, %1, %2" : "=&v"(pfd) : "v"(o), "s"(pc) : "memory");
  }
  int role = wave >= 4 ? 1 : mainwg ? 0 : 2;
  u32x4 xa, xb;
  u32 eps, dAr, dAi, dBr, dBi, dCr, dCi, ldt2, dsk2;
  if (role == 1) {
    __builtin_amdgcn_s_setprio(2);
    stl<u32x2>(lds + LPG, u32x2{0x80808080u, 0x80808080u});
    u32 vo = min(lane, 55) * 16 + ((xcd * 32 + zq * 8 + jc) * 4 + wave - 4) * 1792;
    u32 po = lane * 4 + ((xcd * 28 + zq * 7 + min(jc, 6)) * 4 + wave - 4) * 256, zero = 0;
    asm volatile("" : "+v"(vo), "+v"(po), "+v"(zero));
    SCHED();
    asm volatile("global_load_dwordx4 %0, %3, %4\n\tglobal_load_dwordx4 %1, %3, %4 offset:896\n\tglobal_load_dword %2, %5, %6 sc1"
                 : "=&v"(xa), "=&v"(xb), "=&v"(eps) : "v"(vo), "s"(x), "v"(zero), "s"(ws) : "memory");
    asm volatile("global_load_dword %0, %6, %7\n\tglobal_load_dword %1, %6, %8\n\tglobal_load_dword %2, %6, %9\n\t"
                 "global_load_dword %3, %6, %10\n\tglobal_load_dword %4, %6, %11\n\tglobal_load_dword %5, %6, %12"
                 : "=&v"(dAr), "=&v"(dAi), "=&v"(dBr), "=&v"(dBi), "=&v"(dCr), "=&v"(dCi)
                 : "v"(po), "s"(pAr), "s"(pAi), "s"(pBr), "s"(pBi), "s"(pCr), "s"(pCi) : "memory");
    asm volatile("s_load_dword %0, %2, %4\n\ts_load_dword %1, %3, %4" : "=&s"(ldt2), "=&s"(dsk2) : "s"(pdt), "s"(pds), "s"((xcd * 28 + zq * 7 + min(jc, 6)) * 4) : "memory");
  }
  asm volatile("" : "+s"(role));
  if (role == 2) {
    const int ci = (xcd >> 1) * 4 + zq;
    const u32 vo = (lane >> 3) * 3584 + 16 * ((lane & 7) ^ (((lane >> 4) & 1) << 2)) + (ci < 14 ? 0u : OOB);
#pragma unroll 1
    for (int i = wave * 14; i < wave * 14 + 14; ++i) dma(lb + LCW + i * 1024, vo, RSRC(w1, D * F * 2), i * 8 * 3584 + ((xcd & 1) * 14 + ci) * 128);
#pragma unroll 1
    for (int i = wave; i < 14; i += 4) dma(lb + LCW + 57344 + i * 1024, lane * 16, RSRC(w2, F * D * 2), (zq * 56 + 16 + (xcd >> 1) * 4 + (xcd & 1) + (i >= 7)) * 7168 + i * 1024);
  }
  asm volatile("" : "+s"(role));
  if (role == 0) {
    const int wv = wave, wf_ = wv >> 1, wt = wv & 1;
    const int g1 = (xcd >> 1) * 4 + zq, j1 = (xcd & 1) * 7 + jc, tb = xcd * 4 + zq;
    u32 vW = (lane >> 4) * 3584 + 16 * ((lane & 15) ^ ((lane >> 4) << 1));
    u32 vX0 = (lane >> 3) * 896 + 16 * ((lane & 7) ^ (lane >> 4)), vX1 = vX0 ^ 64;
    u32 sX = (g1 * 4 + wave) * (32 * 896), sW = wave * 16 * 3584 + j1 * 256, lG = lb + wave * 4096;
    asm volatile("" : "+v"(vW), "+v"(vX0), "+v"(vX1));
    asm volatile("" : "+s"(sX), "+s"(sW), "+s"(lG));
    SCHED();
    const rsrc_t rsW = RSRC(ws, WS_END), rs1 = RSRC(w1, D * F * 2), rsx = RSRC(x, L * D * 2);
    const int q4 = (lane & 15) >> 2;
    u32 sV = wv * 448 * 896 + jc * 128, lV = lb + wv * 8192, vV2 = (lane >> 3) * 896 + 16 * ((lane & 7) ^ ((lane >> 4) << 1));
    const rsrc_t rs2 = RSRC(w2, F * D * 2);
    u32 vXi[4], vWi[4];
#pragma unroll
    for (int i = 0; i < 4; ++i) { vXi[i] = ((i & 1) ? vX1 : vX0) + i * (8 * 896 - 1024); vWi[i] = (vW ^ ((i >> 1) * 128)) + i * (4 * 3584 - 1024); }
    auto dman = [&](auto N, auto I) {
      constexpr int n = N, i = I;
      if constexpr (n >= 7) dmai<sbase(wslot(n - 7)) + i * 1024, ((n - 7) * 64 + i * 8) * 896>(lV, vV2, rs2, sV);
      else if constexpr (i == 0) dma0<sbase(n % 4)>(lG, vXi[0], rsx, sX + n * 128);
      else if constexpr (i < 4) dman_<i * 1024>(vXi[i], rsx, sX + n * 128);
      else if constexpr (i == 4) dma0<sbase(n % 4) + 16384>(lG, vWi[0], rs1, sW + n * (64 * 3584));
      else dman_<(i - 4) * 1024>(vWi[i - 4], rs1, sW + n * (64 * 3584));
    };
    __builtin_amdgcn_s_setprio(1);
    sfor<8>([&](auto I) { dman(std::integral_constant<int, 0>{}, I); });
    SCHED();
    u32 ep, b1v;
    u32x2 gc, gs;
    u32 ob_ = (j1 * 128 + wf_ * 64 + lane) * 2, oc_ = WS_CS + ob_ * 4, os_ = WS_ST + (g1 * 128 + wt * 64 + lane) * 8;
    auto ldaug = [&]() {
      asm volatile("buffer_load_dwordx2 %0, %2, %4, 0 offen sc1\n\tbuffer_load_dwordx2 %1, %3, %4, 0 offen sc1"
                   : "=&v"(gc), "=&v"(gs) : "v"(oc_), "v"(os_), "s"(rsW) : "memory");
    };
    const int g4 = lane >> 4;
    u32 aW[2][4], aX[2][2];
#pragma unroll
    for (int t = 0; t < 4; ++t) {
      aW[0][t] = (lb + SLOT + 16384 + (8 * g4 + q4) * 256 + ((wf_ * 128 + (lane & 3) * 8) ^ ((q4 | (g4 & 1) << 2) << 5))) ^ (32 * t);
      aW[1][t] = aW[0][t] + 2 * SLOT;
    }
#pragma unroll
    for (int s = 0; s < 2; ++s) {
      aX[0][s] = (lb + SLOT + (wt * 64 + (lane & 15)) * 128 + 16 * (g4 ^ ((lane >> 1) & 7))) ^ (64 * s);
      aX[1][s] = aX[0][s] + 2 * SLOT;
    }
    asm volatile("" : "+v"(aW[1][0]), "+v"(aW[1][1]), "+v"(aW[1][2]), "+v"(aW[1][3]), "+v"(aX[1][0]), "+v"(aX[1][1]));
    asm volatile("" : "+v"(aW[0][0]), "+v"(aW[0][1]), "+v"(aW[0][2]), "+v"(aW[0][3]), "+v"(aX[0][0]), "+v"(aX[0][1]));
    asm volatile("" : "+v"(ob_), "+v"(oc_), "+v"(os_), "+v"(vV2));
    asm volatile("" : "+s"(sV), "+s"(lV));
    SCHED();
    sfor<12>([&](auto I) { dman(std::integral_constant<int, 1 + I / 8>{}, std::integral_constant<int, I % 8>{}); });
    asm volatile("buffer_load_dword %0, off, %2, 0 sc1\n\tglobal_load_ushort %1, %3, %4"
                 : "=&v"(ep), "=&v"(b1v) : "s"(rsW), "v"(ob_), "s"(b1) : "memory");
    SCHED();
    f32x4 ac1[4][4];
#pragma unroll
    for (int i = 0; i < 16; ++i) ac1[i >> 2][i & 3] = f32x4{0, 0, 0, 0};
    u32x4 af[2][4], xf[2][4];
    auto rd1 = [&](int c, int r) {
      const u32 o = (c & 1) * SLOT;
      const int s = (r >> 2) & 1, t = r & 3;
      if (r < 8) af[s][t] = trfrag((LAS char*)(size_t)aW[((c + 2) >> 1) & 1][t] + o + s * 8192, 1024);
      else xf[s][t] = ldl<u32x4>((LAS char*)(size_t)aX[((c + 2) >> 1) & 1][s] + o + t * 2048);
    };
    auto mm = [&](int m) {
      const int ft = (m >> 2) & 3, tt = (m & 4 ? ~m : m) & 3;
      ac1[ft][tt] = mfma16(af[m >> 4][ft], xf[m >> 4][tt], ac1[ft][tt]);
      SCHED();
    };
    const u32 hdst = WS_H + ((g1 * 2 + wt) * 28 + j1 * 2 + wf_) * 8192 + lane * 16;
    auto epi = [&](int n) {
      const int kb = n >> 2, tt = n & 3;
      float v[8];
#pragma unroll
      for (int e = 0; e < 8; ++e) { v[e] = __int_as_float(max(__float_as_int(ac1[2 * kb + (e >> 2)][tt][e & 3]), 0)); v[e] *= v[e]; }
      stg16(rsW, hdst, n * 1024, u32x4{pk(v[0], v[1]), pk(v[2], v[3]), pk(v[4], v[5]), pk(v[6], v[7])});
      SCHED();
    };
    u32 tag = 0, tv;
    const u32 tfl = WS_TF + (wv * 8 + (lane & 7)) * 128;
    sfor<7>([&](auto K) {
      constexpr int k = K;
      asm volatile("s_waitcnt vmcnt(%0) lgkmcnt(0)" ::"i"(k == 0 ? 14 : k == 1 || k == 6 ? 18 : 16) : "memory");
      if constexpr (k == 0) asm volatile("" ::"v"(pfd));
      if constexpr (k == 2) asm volatile("" : "+v"(ep), "+v"(b1v));
      if constexpr (k > 0) __builtin_amdgcn_s_sleep(1);
      BAR();
      if constexpr (k == 5) ldaug();
      sfor<32>([&](auto N) {
        constexpr int n = N;
        if constexpr (k > 0 || n >= 8) mm((n + 24) & 31);
        if constexpr (gap_read(n) >= 0) rd1(k, gap_read(n));
        constexpr int d = gap_dma(n, k == 0);
        if constexpr (d >= 0 && k < 6) {
          constexpr int p = (k == 0 ? 20 : 24 + 8 * k) + d;
          dman(std::integral_constant<int, p / 8>{}, std::integral_constant<int, p % 8>{});
        }
        if constexpr (k == 6 && n == 24) { post(lds, LPG, wv, 1); tag = __builtin_amdgcn_readfirstlane(ep) * TAG_MUL + TAG_ADD; }
        SCHED();
      });
    });
    {
      __builtin_amdgcn_s_setprio(3);
      asm volatile("s_waitcnt vmcnt(8)" : "+v"(gc), "+v"(gs)::"memory");
#pragma unroll 1
      for (u32 n = 0; n < SPIN_CAP; ++n) {
        if (allof(gc[1] == tag && gs[1] == tag)) break;
        ldaug();
        asm volatile("s_waitcnt vmcnt(0)" : "+v"(gc), "+v"(gs)::"memory");
      }
      const auto h2 = __builtin_amdgcn_permlane32_swap(gs[0], gs[0], false, false);
      const auto r01 = __builtin_amdgcn_permlane16_swap(h2[0], h2[0], false, false), r23 = __builtin_amdgcn_permlane16_swap(h2[1], h2[1], false, false);
      const u32 st[4] = {r01[0], r01[1], r23[0], r23[1]};
      u32x2 aa[4], ab[4];
#pragma unroll
      for (int t = 0; t < 4; ++t) {
        mm(24 + 2 * t);
        aa[t] = g4 == t ? u32x2{gc[0], b1v} : u32x2{0u, 0u};
        SCHED();
        mm(25 + 2 * t);
        ab[t] = u32x2{__builtin_amdgcn_perm(st[t], st[t], 0x01000100u) ^ 0x80008000u, st[t] >> 16};
        SCHED();
      }
#pragma unroll
      for (int m = 0; m < 16; ++m)
        ac1[m >> 2][m & 3] = __builtin_amdgcn_mfma_f32_16x16x16bf16_1k(__builtin_bit_cast(s16x4, aa[m >> 2]), __builtin_bit_cast(s16x4, ab[m & 3]), ac1[m >> 2][m & 3], 0, 0, 0);
      SCHED();
      epi(0); epi(1); epi(2); epi(3); epi(4);
      asm volatile("buffer_load_dword %0, %1, %2, 0 offen sc1" : "=v"(tv) : "v"(tfl), "s"(rsW) : "memory");
      epi(5); epi(6); epi(7);
    }
    __builtin_amdgcn_s_setprio(0);
    WAITV(9);
    u32x4 wf[56];
    u32 aV[4];
#pragma unroll
    for (int t = 0; t < 4; ++t) aV[t] = (lV + sbase(3) + (4 * g4 + q4) * 128 + (((lane & 3) * 8) | (((g4 & 1) << 1 | q4 >> 1) << 5))) ^ (32 * t);
    asm volatile("" : "+v"(aV[0]), "+v"(aV[1]), "+v"(aV[2]), "+v"(aV[3]));
    auto rdw = [&](int m) { wf[m] = trfrag((LAS char*)(size_t)aV[m & 3] + (sbase(wslot(m >> 3)) - sbase(3)) + (m & 4) * 1024, 2048); };
#pragma unroll
    for (int m = 0; m < 16; ++m) rdw(m);
#pragma unroll
    for (int m = 0; m < 16; m += 8)
      asm volatile("" : "+v"(wf[m]), "+v"(wf[m + 1]), "+v"(wf[m + 2]), "+v"(wf[m + 3]), "+v"(wf[m + 4]), "+v"(wf[m + 5]), "+v"(wf[m + 6]), "+v"(wf[m + 7]));
    u32 hfl = WS_HF + (tb * 32 + wv * 7 + min(lane, 6)) * 16, hfo = lane ? OOB : WS_HF + ((g1 * 2 + wt) * 32 + j1 * 2 + wf_) * 16;
    asm volatile("" : "+v"(hfl), "+v"(hfo));
    WAITV(0);
    __builtin_amdgcn_raw_buffer_store_b32(tag, rsW, hfo, 0, 16);
    asm volatile("" : "+v"(tv));
#pragma unroll 1
    for (u32 n = 0; n < SPIN_CAP; ++n) {
      if (allof(tv == tag)) break;
      asm volatile("buffer_load_dword %0, %1, %2, 0 offen sc1\n\ts_waitcnt vmcnt(0)" : "=&v"(tv) : "v"(tfl), "s"(rsW) : "memory");
    }
    FENCE();
#pragma unroll
    for (int i = 0; i < 16; ++i) wf[16 + i] = ldg16(rsW, lane * 16, WS_W2T + (jc * 4 + wv) * TSTR + i * 1024);
    SCHED();
    await<3>(lds, LPS);
    {
      u32 fv;
      auto dchunk = [&](auto N) { sfor<8>([&](auto I) { dman(N, I); }); };
      dchunk(std::integral_constant<int, 11>{}); dchunk(std::integral_constant<int, 12>{});
      asm volatile("buffer_load_dword %0, %1, %2, 0 offen sc1" : "=v"(fv) : "v"(hfl), "s"(rsW) : "memory");
      dchunk(std::integral_constant<int, 13>{});
      asm volatile("s_waitcnt vmcnt(8)" : "+v"(fv)::"memory");
#pragma unroll 1
      for (u32 n = 0; n < SPIN_CAP; ++n) {
        if (allof(fv == tag)) break;
        asm volatile("buffer_load_dword %0, %1, %2, 0 offen sc1\n\ts_waitcnt vmcnt(0)" : "=&v"(fv) : "v"(hfl), "s"(rsW) : "memory");
      }
      FENCE();
    }
    const u32 hsrc = WS_H + (tb * 28 + wv * 7) * 8192;
    u32x4 hf[56];
    auto ld1 = [&](int n) {
      if (n < 56) hf[hord(n)] = ldg16(rsW, lane * 16, hsrc + hord(n) * 1024);
      SCHED();
    };
    f32x4 ac2[4][4];
#pragma unroll
    for (int i = 0; i < 16; ++i) ac2[i >> 2][i & 3] = f32x4{0, 0, 0, 0};
#pragma unroll
    for (int n = 0; n < 10; ++n) ld1(n);
    u32 pw[2];
    auto stp = [&](int n) {
      const int ct = n & 3, tt = n >> 2;
      stl<f32x4>((LAS char*)(size_t)pw[ct & 1] + (ct >> 1) * 128 + (tt & 1) * 4096 + (tt >> 1) * SLOT, ac2[ct][tt]);
      SCHED();
    };
    sfor<224>([&](auto M) {
      constexpr int n = M / 4, ct = M % 4, f = hord(n);
      constexpr int c = n & 1 ? 3 - ct : ct;
      ac2[c][f & 3] = mfma16(wf[(f & ~3) + c], hf[f], ac2[c][f & 3]);
      SCHED();
      if constexpr (ct == 0 && n > 0) ld1(n < 5 ? n + 9 : n < 8 ? 2 * n + 4 : n + 12);
      if constexpr (ct == 2 && n >= 5 && n < 8) ld1(2 * n + 5);
      if constexpr (ct == 1 && n >= 18 && n < 42) rdw(n + 14);
      if constexpr ((ct & 1) && n >= 50 && n < 54) stp(2 * (n - 50) + (ct >> 1));
      if constexpr (ct == 3 && n == 33) post(lds, LPG, wv, 2);
      if constexpr (ct == 3 && n == 44) {
        LANES();
        u32 p0 = lb + sbase(0) + wv * 8192 + (lane & 15) * 256 + (((lane >> 4) ^ lane) & 7) * 16, p1 = p0 ^ 64;
        asm volatile("" : "+v"(p0), "+v"(p1));
        pw[0] = p0; pw[1] = p1;
      }
      if constexpr (ct == 3 && n == 53) post(lds, LPG, wv, 3);
      SCHED();
    });
#pragma unroll
    for (int n = 8; n < 16; ++n) stp(n);
    post(lds, LPG, wv, 4);
    DIE();
  }
  asm volatile("" : "+s"(role));
  if (role == 1) {
    LANES();
    const int h = lane >> 5, sw = wave & 3, cl = sw >> 1, hf2 = sw & 1;
    const int ch = (xcd * 28 + zq * 7 + min(jc, 6)) * 2 + cl, tg = xcd * 32 + zq * 8 + jc, tb = xcd * 4 + zq;
    const u32 vo56 = lane < 56 ? lane * 16 : OOB;
    const rsrc_t rsW = RSRC(ws, WS_END), rsx = RSRC(x, L * D * 2);
    LAS char* stg = lds + LSTG + sw * STG_W;
    SCHED();
    LAS char* kc = lds + LKC + cl * KC_CH;
    LAS char* ub = lds + LU + cl * U_CH;
    LAS char* rp = stg + (lane & 15) * STG_R + (lane >> 4) * 16;
    const u32 xfl = WS_XF + ((xcd >> 1) * 256 + sw * 64 + lane) * 8, xfo = lane ? OOB : WS_XF + (sw * 256 + tg) * 8;
    u32 xfw = lane == (sw ? 64 : 1) ? WS_WF + tg * 32 : xfo;
    asm volatile("" : "+v"(xfw));
    SCHED();
    const int n0 = hf2 * 64 + lane;
    u32 vi[11], va0, va1, va3, ka, ua, kw, uw;
    auto idxa = [&]() {
      const u32 c0 = n0 < 32 ? 0x3456789Au : hf2 ? 0x33344455u : 0x33455567u;
      const u32 c1 = n0 == 0 ? 0x333u : n0 <= 32 ? 0x012u : n0 <= 64 ? 0x123u : 0x233u;
      const u32 vb_ = lb + LSTG + cl * 2 * STG_W - 12 + min(min(n0 * 32, 512 + n0 * 16), 1024 + n0 * 8);
#pragma unroll
      for (int e = 0; e < 11; ++e) {
        u32 t = __builtin_amdgcn_ubfe(e < 8 ? c0 : c1, 4 * (e & 7), 4);
        asm volatile("" : "+v"(t));
        vi[e] = vb_ + (t << 2);
      }
      asm volatile("" : "+v"(vi[0]), "+v"(vi[1]), "+v"(vi[2]), "+v"(vi[3]), "+v"(vi[4]), "+v"(vi[5]), "+v"(vi[6]), "+v"(vi[7]), "+v"(vi[8]), "+v"(vi[9]), "+v"(vi[10]));
    };
    auto idxb = [&]() {
      const int grp = lane >> 4, col = lane & 15;
      const u32 sh = min(5, 6 - grp);
      va0 = lb + LSTG + sw * STG_W + (col << sh) + (grp << (grp < 2 ? 4 : 9));
      va1 = va0 + (16u << sh);
      va3 = lb + LSTG + sw * STG_W + 2048 + grp * 512 + col * 4;
      ka = lb + LKC + cl * KC_CH + (3 - (col & 3)) * KC_CP + (2028 - 1792 - 96 - 128 * hf2 - (col & 12) + 8 * grp) * 2;
      ua = lb + LU + cl * U_CH + 512 + (16 * col - 128 * hf2 - 16 - 96 + 8 * grp) * 2;
      kw = lb + LKC + cl * KC_CH + (127 - n0) * 16;
      uw = lb + LU + (lane & 1) * U_CH + 512 + (sw * 64 + (lane >> 1)) * 16;
      asm volatile("" : "+v"(va0), "+v"(va1), "+v"(va3), "+v"(ka), "+v"(ua), "+v"(kw), "+v"(uw));
    };
    idxb();
    asm volatile("s_waitcnt lgkmcnt(0)" : "+s"(ldt2), "+s"(dsk2)::"memory");
    float dt = rbf(expf(cl ? hi16(ldt2) : lo16(ldt2)));
    asm volatile("" : "+v"(dt));
    SCHED();
    asm volatile("s_waitcnt vmcnt(6)" : "+v"(xa), "+v"(xb), "+v"(eps) : "v"(pfd) : "memory");
    const u32 tag = __builtin_amdgcn_readfirstlane(eps) * TAG_MUL + TAG_ADD;
    {
      float s0 = 0.f, q0 = 0.f, s1 = 0.f, q1 = 0.f;
#pragma unroll
      for (int i = 0; i < 4; ++i) {
        s0 = dot2(xa[i], 0x3F803F80u, s0); q0 = dot2(xa[i], xa[i], q0);
        s1 = dot2(xb[i], 0x3F803F80u, s1); q1 = dot2(xb[i], xb[i], q1);
      }
      if (lane >= 56) { s0 = 0.f; q0 = 0.f; s1 = 0.f; q1 = 0.f; }
      float v = addswap16(addswap32(s0, s1), addswap32(q0, q1));
      v = dppadd<0xB1>(v); v = dppadd<0x4E>(v); v = dppadd<0x141>(v); v = dppadd<0x140>(v);
      v *= 1.0f / D;
      auto r = __builtin_amdgcn_permlane16_swap(__float_as_uint(v), __float_as_uint(v), false, false);
      const float mean = __uint_as_float(r[0]);
      const float var = rbf(fmaxf(__builtin_fmaf(-mean, mean, __uint_as_float(r[1])), 0.f));
      const u32 st = pk(mean, __builtin_amdgcn_sqrtf(rbf(var + 1e-5f)));
      asm volatile("s_waitcnt vmcnt(0)" : "+v"(dAr), "+v"(dAi), "+v"(dBr), "+v"(dBi), "+v"(dCr), "+v"(dCi)::"memory");
      __builtin_amdgcn_raw_buffer_store_b64(u32x2{st, tag}, rsW, (lane & 31) ? OOB : WS_ST + (tg * 8 + sw * 2 + h) * 8, 0, 16);
      const float rs = __builtin_amdgcn_rcpf(hi16(st)), ms = -lo16(st) * rs;
      auto rr = __builtin_amdgcn_permlane32_swap(__float_as_uint(rs), __float_as_uint(rs), false, false);
      auto mr = __builtin_amdgcn_permlane32_swap(__float_as_uint(ms), __float_as_uint(ms), false, false);
      const float r0 = __uint_as_float(rr[0]), r1 = __uint_as_float(rr[1]), m0 = __uint_as_float(mr[0]), m1 = __uint_as_float(mr[1]);
      u32 o[8];
#pragma unroll
      for (int i = 0; i < 4; ++i) {
        o[2 * i] = pk(__builtin_fmaf(lo16(xa[i]), r0, m0), __builtin_fmaf(lo16(xb[i]), r1, m1));
        o[2 * i + 1] = pk(__builtin_fmaf(hi16(xa[i]), r0, m0), __builtin_fmaf(hi16(xb[i]), r1, m1));
      }
      stl<u32x4>(lds + LSTG + sw * 2048 + lane * 32, u32x4{o[0], o[1], o[2], o[3]});
      stl<u32x4>(lds + LSTG + sw * 2048 + lane * 32 + 16, u32x4{o[4], o[5], o[6], o[7]});
    }
    auto xtout = [&](int i) {
      LAS char* p = lds + LSTG + (sw * 112 + i * 56 + lane) * 4;
      stg16(rsW, vo56, WS_XT + tg * XSTR + (sw * 112 + i * 56) * 16, u32x4{ldl<u32>(p), ldl<u32>(p + 2048), ldl<u32>(p + 4096), ldl<u32>(p + 6144)});
    };
    if (!mainwg) {
      stl<u32>(lds + LTAG, tag);
      asm volatile("s_waitcnt lgkmcnt(0)" ::: "memory");
      BAR();
      xtout(0); xtout(1);
      if (sw == 0 && lane == 0) stflag(ws, WS_WF + tg * 32, tag);
      WAITV(0);
      __builtin_amdgcn_raw_buffer_store_b32(tag, rsW, xfo, 0, 16);
      const rsrc_t rs2 = RSRC(w2, F * D * 2);
      const u32 ln = (((zq * 8 + xcd) * 4 + sw) * 64 + lane) * 128;
      const u32 t_ = __builtin_amdgcn_raw_buffer_load_b32(rs2, ln, 0, 0) ^ __builtin_amdgcn_raw_buffer_load_b32(rs2, ln, 8192 * 128, 0);
      asm volatile("" ::"v"(t_));
      EXIT();
    }
    BAR();
    __builtin_amdgcn_s_setprio(0);

    u32 rw[2][53];
    u32 xfv = 0;
    const int tok0 = tb * 64 + sw * 8, oc = lane & 7;
    const u32 oo = ((tok0 + (lane >> 3)) * D + jc * 64 + oc * 8) * 2, so = WS_ST + (tok0 + (lane >> 3)) * 8;
    u32x4 uv[2], xv[2], bv;
    u32x2 sv[2];
    auto ldu = [&]() {
      FENCE();
      const u32 us = WS_XT + sw * 64 * XSTR + (ch & ~1) * 16;
      uv[0] = ldg16(rsW, (lane >> 1) * XSTR + (lane & 1) * 16, us);
      uv[1] = ldg16(rsW, (lane >> 1) * XSTR + (lane & 1) * 16, us + 32 * XSTR);
    };
    auto ldo = [&]() {
#pragma unroll
      for (int it = 0; it < 2; ++it) {
        xv[it] = __builtin_amdgcn_raw_buffer_load_b128(rsx, oo, it * 32 * D * 2, 0);
        sv[it] = __builtin_amdgcn_raw_buffer_load_b64(rsW, so, it * 256, 16);
      }
      bv = *(const u32x4*)((const char*)b2 + (u32)(jc * 128 + oc * 16));
      SCHED();
    };
    u32x4 fr[2][4][4];
    f32x4 k00 = {0, 0, 0, 0}, k01 = k00, k10 = k00, k11 = k00;
    auto stage = [&](auto P, auto TL) {
      constexpr int p = P, tl = TL;
      sfor<(tl < 3 ? 16 : 5)>([&](auto R) {
        constexpr int r = R;
        const u32 v = rw[p][16 * tl + r];
        asm volatile("ds_write_addtid_b32 %0 offset:%1" ::"v"(v), "i"(r * STG_R) : "memory");
      });
#pragma unroll
      for (int s = 0; s < 4; ++s) fr[p][tl][s] = ldl<u32x4>(rp + s * 64);
      FENCE();
      SCHED();
    };
    u32 zh[11][2];
    float kscale;
    {
      cpx z[2][2], c[2];
#pragma unroll
      for (int p = 0; p < 2; ++p) {
        auto up = [&](u32 d) { return p ? hi16(d) : lo16(d); };
        const float a2 = (dt * up(dAr)) * 1.44269504088896f, b2r = (dt * up(dAi)) * 0.15915494309189535f;
        z[p][0] = cpow(a2, b2r, 1.f); z[p][1] = cpow(a2, b2r, 32.f);
      }
      asm volatile("" : "+v"(z[0][0].r), "+v"(z[0][1].r), "+v"(z[1][0].r), "+v"(z[1][1].r));
      asm volatile("" : "+v"(z[0][0].i), "+v"(z[0][1].i), "+v"(z[1][0].i), "+v"(z[1][1].i));
#pragma unroll
      for (int i = 0; i < 6; ++i) {
#pragma unroll
        for (int p = 0; p < 2; ++p) { if (i < 5) zh[i][p] = pkh(z[p][0]); zh[5 + i][p] = pkh(z[p][1]); }
        if (i < 5) {
          float t[4], u[4];
#pragma unroll
          for (int n = 0; n < 4; ++n) { const cpx v = z[n >> 1][n & 1]; t[n] = v.i * v.i; u[n] = v.r + v.r; }
          asm volatile("" : "+v"(t[0]), "+v"(t[1]), "+v"(t[2]), "+v"(t[3]), "+v"(u[0]), "+v"(u[1]), "+v"(u[2]), "+v"(u[3]));
#pragma unroll
          for (int n = 0; n < 4; ++n) { const cpx v = z[n >> 1][n & 1]; t[n] = __builtin_fmaf(v.r, v.r, -t[n]); u[n] *= v.i; }
          asm volatile("" : "+v"(t[0]), "+v"(t[1]), "+v"(t[2]), "+v"(t[3]), "+v"(u[0]), "+v"(u[1]), "+v"(u[2]), "+v"(u[3]));
#pragma unroll
          for (int n = 0; n < 4; ++n) z[n >> 1][n & 1] = cpx{t[n], u[n]};
        }
        if (i == 1) {
          asm volatile("s_waitcnt lgkmcnt(0)" ::: "memory");
          BAR();
          HI(); xtout(0); xtout(1); LO();
        }
      }
#pragma unroll
      for (int p = 0; p < 2; ++p) {
        auto up = [&](u32 d) { return p ? hi16(d) : lo16(d); };
        c[p] = cmul(cpx{up(dCr), -up(dCi)}, cpx{up(dBr), -up(dBi)});
      }
      asm volatile("" : "+v"(c[0].r), "+v"(c[1].r));
      asm volatile("" : "+v"(c[0].i), "+v"(c[1].i));
      float m = fmaxf(fmaxf(fabsf(c[0].r), fabsf(c[0].i)), fmaxf(fabsf(c[1].r), fabsf(c[1].i)));
      m = dppmax<0xB1>(m); m = dppmax<0x4E>(m); m = dppmax<0x141>(m); m = dppmax<0x140>(m);
      const u32 mb = max(max(__builtin_amdgcn_readlane(__float_as_uint(m), 0), __builtin_amdgcn_readlane(__float_as_uint(m), 16)),
                         max(__builtin_amdgcn_readlane(__float_as_uint(m), 32), __builtin_amdgcn_readlane(__float_as_uint(m), 48)));
      const u32 eb = min(max(mb & 0x7f800000u, 16u << 23), 240u << 23);
      const float up_ = __uint_as_float((262u << 23) - eb);
      kscale = dt * __uint_as_float(eb - (8u << 23));
#pragma unroll
      for (int p = 0; p < 2; ++p) {
        float r = c[p].r * up_, i = c[p].i * up_;
        asm volatile("" : "+v"(r), "+v"(i));
        rw[p][32] = pkh(cpx{r, i});
        rw[p][0] = 0x3C00u; rw[p][1] = zh[3][p]; rw[p][2] = zh[4][p]; rw[p][4] = zh[5][p]; rw[p][8] = zh[6][p]; rw[p][16] = zh[7][p];
      }
    }
    asm volatile("" : "+v"(rw[0][32]), "+v"(rw[1][32]), "+v"(kscale));
    BAR();
    hprod<false, 1>(rw, 3, {1}, zh[4]);
    hprod<true, 1>(rw, 33, {32}, zh[0]);
    hprod<false, 3>(rw, 5, {1, 2, 3}, zh[5]);
    WAITV(0);
    HI();
    __builtin_amdgcn_raw_buffer_store_b32(tag, rsW, xfw, 0, 16);
    LO();
    hprod<true, 2>(rw, 34, {32, 33}, zh[1]);
    hprod<true, 4>(rw, 36, {32, 33, 34, 35}, zh[2]);
    BAR();
    hprod<false, 7>(rw, 9, {1, 2, 3, 4, 5, 6, 7}, zh[6]);
    hprod<true, 4>(rw, 40, {32, 34, 36, 38}, zh[8]);
    hprod<true, 4>(rw, 44, {32, 36, 40, 42}, zh[9]);
    BAR();
    hprod<true, 4>(rw, 48, {32, 40, 44, 46}, zh[10]);
    hprod<false, 7>(rw, 17, {1, 2, 3, 4, 5, 6, 7}, zh[7]);
    hprod<true, 1>(rw, 52, {48}, zh[10]);
    BAR();
    HI(); xfv = __builtin_amdgcn_raw_buffer_load_b32(rsW, xfl, 0, 16); LO();
    hprod<false, 8>(rw, 24, {8, 9, 10, 11, 12, 13, 14, 15}, zh[7]);
    idxa();
    asm volatile("s_mov_b32 m0, %0" ::"s"(lb + LSTG + sw * STG_W) : "memory");
    const u32 pairm = 0x8080u << (16 * cl);
    BAR();
    HI();
#pragma unroll 1
    for (u32 n = 0; n < SPIN_CAP; ++n) {
      if (allof(xfv == tag)) break;
      FENCE();
      xfv = __builtin_amdgcn_raw_buffer_load_b32(rsW, xfl, 0, 16);
    }
    ldu();
    ldo();
    sfor<4>([&](auto T) { stage(std::integral_constant<int, 0>{}, T); });
    const u32 pvg = peek(lds, LPG);
    stage(std::integral_constant<int, 1>{}, std::integral_constant<int, 0>{});
    await<1>(lds, LPG, pvg, 0x80808080u);
    stl<u32>(kc + (lane >> 4) * KC_CP + 4096 + (lane & 15) * 4, 0u);
    stl<u32>(ub + (hf2 * 64 + lane) * 4, 0u);
    sfor<3>([&](auto T) { stage(std::integral_constant<int, 1>{}, std::integral_constant<int, T + 1>{}); });
    stl<u32x4>((LAS char*)(size_t)uw, uv[0]);
    stl<u32x4>((LAS char*)(size_t)uw + 512, uv[1]);
    SCHED();
#pragma unroll
    for (int p = 0; p < 2; ++p)
#pragma unroll
      for (int s = 0; s < 4; ++s) {
        k00 = mfma16h(fr[p][2][s], fr[p][0][s], k00); k01 = mfma16h(fr[p][2][s], fr[p][1][s], k01);
        k10 = mfma16h(fr[p][3][s], fr[p][0][s], k10); k11 = mfma16h(fr[p][3][s], fr[p][1][s], k11);
      }
    {
      const u32 d01 = lane >= 48 ? 256 : 8;
      k00 *= kscale; k01 *= kscale; k10 *= kscale; k11 *= kscale;
      stl<f32x2>((LAS char*)(size_t)va0, f32x2{k00[0], k00[1]}); stl<f32x2>((LAS char*)(size_t)(va0 + d01), f32x2{k00[2], k00[3]});
      stl<f32x2>((LAS char*)(size_t)va1, f32x2{k01[0], k01[1]}); stl<f32x2>((LAS char*)(size_t)(va1 + d01), f32x2{k01[2], k01[3]});
#pragma unroll
      for (int r = 0; r < 4; ++r) { stl<float>((LAS char*)(size_t)va3 + 128 * r, k10[r]); stl<float>((LAS char*)(size_t)va3 + 128 * r + 64, k11[r]); }
    }
    post(lds, LPS, sw, 2);
    await<2>(lds, LPS, pairm);
    {
      float k0[14], k1[14];
      LAS char* v3 = lds + LSTG + cl * 2 * STG_W + (511 + n0) * 4;
#pragma unroll
      for (int e = 0; e < 14; ++e) {
        LAS char* p = e < 11 ? (LAS char*)(size_t)vi[e] : v3 + (e - 11) * 4;
        k0[e] = ldl<float>(p); k1[e] = ldl<float>(p + STG_W);
      }
      SCHED();
      float kv[14];
#pragma unroll
      for (int e = 0; e < 14; ++e) { kv[e] = k0[e] + k1[e]; asm volatile("" : "+v"(kv[e])); }
      const float vb = kv[11], va = kv[12], vc = kv[13];
      if (n0 == 0) { kv[7] += cl ? hi16(dsk2) : lo16(dsk2); kv[8] = 0.f; kv[9] = 0.f; kv[10] = 0.f; }
      LAS char* p = (LAS char*)(size_t)kw + 2048;
#pragma unroll
      for (int s = 0; s < 4; ++s)
        stl<u32x4>(p + s * KC_CP, u32x4{pk(kv[s], kv[s + 1]), pk(kv[s + 2], kv[s + 3]), pk(kv[s + 4], kv[s + 5]), pk(kv[s + 6], kv[s + 7])});
      const float vs = (lane & 1) ? vc : va;
      const u32 aa = pk(va, va), cc = pk(vc, vc), cs = pk(vc, vs), sa = pk(vs, va), al = pk(va, n0 == 0 ? vb : va);
      p -= 2048;
      stl<u32x4>(p, u32x4{cc, cs, aa, aa});
      stl<u32x4>(p + KC_CP, u32x4{cc, sa, aa, aa});
      stl<u32x4>(p + 2 * KC_CP, u32x4{cs, aa, aa, aa});
      stl<u32x4>(p + 3 * KC_CP, u32x4{sa, aa, aa, al});
    }
    post(lds, LPS, sw, 3);
    await<3>(lds, LPS);
    f32x4 y[8];
#pragma unroll
    for (int q = 0; q < 8; ++q) y[q] = f32x4{0, 0, 0, 0};
    {
      u32x4 A[2][8], B[2][8];
      auto rdc = [&](int m, int p) {
        const u32x2 lo = *(volatile LAS u32x2*)((LAS char*)(size_t)ka + (3 - m) * 64 + (7 - p) * 512), hi = *(volatile LAS u32x2*)((LAS char*)(size_t)ka + (3 - m) * 64 + (7 - p) * 512 + 8);
        A[m & 1][p] = u32x4{lo[0], lo[1], hi[0], hi[1]};
        B[m & 1][p] = ldl<u32x4>((LAS char*)(size_t)ua + (3 - m) * 64 + p * 512);
      };
#pragma unroll
      for (int p = 0; p < 8; ++p) rdc(0, p);
      SCHED();
#pragma unroll
      for (int m = 0; m < 4; ++m) {
        int cnt = 0;
#pragma unroll
        for (int d = 0; d < 8; ++d)
#pragma unroll
          for (int o = 0; o <= d; ++o) {
            if (d + o < 8) {
              y[d + o] = mfma16(A[m & 1][d], B[m & 1][o], y[d + o]);
              if (m < 3 && (++cnt & 3) == 0 && cnt <= 32) { rdc(m + 1, (cnt >> 2) - 1); SCHED(); }
              if (o < d) {
                y[d + o] = mfma16(A[m & 1][o], B[m & 1][d], y[d + o]);
                if (m < 3 && (++cnt & 3) == 0 && cnt <= 32) { rdc(m + 1, (cnt >> 2) - 1); SCHED(); }
              }
            }
          }
        SCHED();
      }
    }
#pragma unroll 1
    for (u32 n = 0; n < SPIN_CAP; ++n) {
      if (allof(sv[0][1] == tag && sv[1][1] == tag)) break;
      FENCE();
      sv[0] = __builtin_amdgcn_raw_buffer_load_b64(rsW, so, 0, 16); sv[1] = __builtin_amdgcn_raw_buffer_load_b64(rsW, so, 256, 16);
    }
    u32 d[4][2];
    auto exch = [&](auto O) {
      constexpr int o = O;
#pragma unroll
      for (int q = 0; q < 4; ++q) stl<f32x4>(stg + q * 1024 + lane * 16, y[(4 - o) + q]);
      post(lds, LPS, sw, 4);
      await<4>(lds, LPS, pairm);
      LAS char* p = lds + LSTG + (sw ^ 1) * STG_W + lane * 16;
      f32x4 v[4];
#pragma unroll
      for (int q = 0; q < 4; ++q) v[q] = ldl<f32x4>(p + q * 1024);
      SCHED();
#pragma unroll
      for (int q = 0; q < 4; ++q) {
        float e[4];
#pragma unroll
        for (int i = 0; i < 4; ++i) e[i] = v[q][i] + y[o + q][i];
        asm volatile("" : "+v"(e[0]), "+v"(e[1]), "+v"(e[2]), "+v"(e[3]) : "i"(o));
        d[q][0] = pk(e[0], e[1]); d[q][1] = pk(e[2], e[3]);
      }
    };
    if (hf2) exch(std::integral_constant<int, 4>{}); else exch(std::integral_constant<int, 0>{});
#pragma unroll
    for (int qp = 0; qp < 2; ++qp) {
      auto r0 = __builtin_amdgcn_permlane16_swap(d[2 * qp][0], d[2 * qp + 1][0], false, false);
      auto r1 = __builtin_amdgcn_permlane16_swap(d[2 * qp][1], d[2 * qp + 1][1], false, false);
      stg16(rsW, lane * 16, WS_YA + ch * YSTR + hf2 * 2048 + qp * 1024, u32x4{r0[0], r1[0], r0[1], r1[1]});
    }
    float bb[2][8], s2[2];
#pragma unroll
    for (int it = 0; it < 2; ++it) {
#pragma unroll
      for (int e = 0; e < 8; ++e)
        bb[it][e] = ((e & 1) ? hi16(xv[it][e >> 1]) : lo16(xv[it][e >> 1])) + ((e & 1) ? hi16(bv[e >> 1]) : lo16(bv[e >> 1]));
      const float sd = hi16(sv[it][0]);
      s2[it] = __builtin_amdgcn_rcpf(sd * sd);
      asm volatile("" : "+v"(bb[it][0]), "+v"(bb[it][1]), "+v"(bb[it][2]), "+v"(bb[it][3]), "+v"(bb[it][4]), "+v"(bb[it][5]), "+v"(bb[it][6]), "+v"(bb[it][7]), "+v"(s2[it]));
    }
    u32 yfl = WS_YF + ((tb >> 4) * 512 + jc * 64 + sw * 16 + (lane & 15)) * 8;
    u32 yo = (lane >> 3) * YSTR + ((lane >> 2) & 1) * 512 + (lane & 3) * 16;
    u32 yw = lb + LYT + sw * 2 * YT_O + (lane >> 3) * 128 + (lane & 3) * 32 + ((lane >> 2) & 1) * 16;
    u32 yr = lb + LYT + oc * YT_O + (sw * 8 + (lane >> 3)) * 2;
    u32 pr0 = lb + sbase(0) + (sw * 8 + (lane >> 3)) * 256 + ((2 * oc) ^ (lane >> 3)) * 16, pr1 = pr0 ^ 16;
    asm volatile("" : "+v"(yfl), "+v"(yo), "+v"(yw), "+v"(yr), "+v"(pr0), "+v"(pr1));
    WAITV(0);
    __builtin_amdgcn_raw_buffer_store_b32(tag, rsW, lane ? OOB : WS_YF + (hf2 * 512 + ch) * 8, 0, 16);
    await<4>(lds, LPS);
    await<2>(lds, LPG);
    {
#pragma unroll 1
      for (u32 n = 0; n < SPIN_CAP; ++n)
        if (allof(__builtin_amdgcn_raw_buffer_load_b32(rsW, yfl, 0, 16) == tag)) break; else { FENCE(); __builtin_amdgcn_s_sleep(8); }
      FENCE();
      const u32 ys = WS_YA + (jc * 64 + sw * 16) * YSTR + (tb >> 3) * 1024 + ((tb >> 2) & 1) * 256 + (tb & 3) * 64;
      const u32x4 t0 = ldg16(rsW, yo, ys), t1 = ldg16(rsW, yo, ys + 8 * YSTR);
      stl<u32x4>((LAS char*)(size_t)yw, t0);
      stl<u32x4>((LAS char*)(size_t)yw + YT_O, t1);
      post(lds, LPS, sw, 5);
      await<5>(lds, LPS);
      u32 yv[2][8];
#pragma unroll
      for (int it = 0; it < 2; ++it)
#pragma unroll
        for (int e = 0; e < 8; ++e) yv[it][e] = ldl<u16>((LAS char*)(size_t)yr + e * 128 + it * 64);
      SCHED();
#pragma unroll
      for (int it = 0; it < 2; ++it) {
#pragma unroll
        for (int e = 0; e < 8; ++e) bb[it][e] += lo16(yv[it][e]);
        asm volatile("" : "+v"(bb[it][0]), "+v"(bb[it][1]), "+v"(bb[it][2]), "+v"(bb[it][3]), "+v"(bb[it][4]), "+v"(bb[it][5]), "+v"(bb[it][6]), "+v"(bb[it][7]));
      }
    }
#pragma unroll
    for (int it = 0; it < 2; ++it) {
      if (it) await<4>(lds, LPG); else await<3>(lds, LPG);
      LAS char *p0 = (LAS char*)(size_t)pr0 + it * SLOT, *p1 = (LAS char*)(size_t)pr1 + it * SLOT;
      const f32x4 a = (ldl<f32x4>(p0) + ldl<f32x4>(p0 + 8192)) + (ldl<f32x4>(p0 + 16384) + ldl<f32x4>(p0 + 24576));
      const f32x4 b = (ldl<f32x4>(p1) + ldl<f32x4>(p1 + 8192)) + (ldl<f32x4>(p1 + 16384) + ldl<f32x4>(p1 + 24576));
      u32x4 o;
#pragma unroll
      for (int e = 0; e < 2; ++e) {
        o[e] = pk(__builtin_fmaf(a[2 * e], s2[it], bb[it][2 * e]), __builtin_fmaf(a[2 * e + 1], s2[it], bb[it][2 * e + 1]));
        o[2 + e] = pk(__builtin_fmaf(b[2 * e], s2[it], bb[it][4 + 2 * e]), __builtin_fmaf(b[2 * e + 1], s2[it], bb[it][5 + 2 * e]));
      }
      stg16(RSRC(out, L * D * 2), oo, it * 32 * D * 2, o);
    }
    DIE();
  }

  {
    LANES();
    const int h = lane >> 5, q4 = (lane & 15) >> 2;
    const u32 aq = ((lane >> 4) & 1) * 32 + (lane & 3) * 8;
    const int ci = (xcd >> 1) * 4 + zq, cb = (xcd & 1) * 14 + ci;
    const rsrc_t rsW = RSRC(ws, WS_END);
    asm volatile("s_waitcnt lgkmcnt(0)\n\ts_barrier\n\ts_waitcnt vmcnt(3)" ::"v"(pfd) : "memory");
    const u32 tag = __builtin_amdgcn_readfirstlane(ldl<u32>(lds + LTAG));
    {
      const u32 s0 = (lane & 15) == 4 * ((lane >> 4) & 1) ? 0x3F803F80u : 0u, s1 = (lane & 15) == 8 + 4 * ((lane >> 4) & 1) ? 0x3F803F80u : 0u;
      LAS char* p = lds + LCW + wave * 14336 + (8 * h + q4) * 128;
      const u32 a = aq ^ ((q4 >> 1) * 64);
      f32x4 c = {0, 0, 0, 0};
#pragma unroll
      for (int s = 0; s < 7; ++s) {
        c = mfma16(u32x4{s0, s0, s0, s0}, trfrag(p + s * 2048 + a, 512), c);
        c = mfma16(u32x4{s1, s1, s1, s1}, trfrag(p + s * 2048 + (a ^ 64), 512), c);
      }
      stl<float>(lds + LCP + wave * 256 + lane * 4, c[0]);
      WAITV(0);
      post(lds, LPG, wave, 1);
      await<1>(lds, LPG);
      if (wave == 0 && ci < 14) {
        LAS char* w = lds + LCP + lane * 4;
        const float cs = (ldl<float>(w) + ldl<float>(w + 256)) + (ldl<float>(w + 512) + ldl<float>(w + 768));
        __builtin_amdgcn_raw_buffer_store_b64(u32x2{pk(cs, cs - rbf(cs)), tag}, rsW, WS_CS + (cb * 64 + lane) * 8, 0, 16);
      }
    }
    {
      LAS char* p = lds + LCW + 57344 + (4 * h + q4) * 896 + aq;
#pragma unroll 1
      for (int t = wave; t < 14; t += 4)
        stg16(rsW, ((lane >> 4) & 1) * 1024 + h * 256 + (lane & 15) * 16, WS_W2T + ((t >> 1) * 4 + zq) * TSTR + (xcd >> 1) * 4096 + (t & 1) * 2048 + (xcd & 1) * 512, trfrag(p + t * 64, 8 * 896));
      WAITV(0);
      post(lds, LPG, wave, 2);
      if (wave == 0) {
        await<2>(lds, LPG);
        if (lane == 0) stflag(ws, WS_TF + (zq * 8 + xcd) * 128, tag);
      }
    }
    if (xcd == 7 && zq == 3 && wave == 0) {
#pragma unroll 1
      for (u32 n = 0; n < SPIN_CAP; ++n) {
        __builtin_amdgcn_s_sleep(64);
        bool ok = true;
#pragma unroll
        for (int i = 0; i < 4; ++i) ok &= ldflag(ws, WS_WF + (i * 64 + lane) * 32) == tag;
        if (allof(ok)) break;
      }
      if (lane == 0) stflag(ws, WS_EPOCH, tag);
    }
    asm volatile(".Lcode_last:");
  }
}

}

extern "C" void kernel_launch(void* const* d_in, const int* in_sizes, int n_in, void* d_out, int out_size, void* d_ws,
                              size_t ws_size, hipStream_t stream) {
  fused_kernel<<<dim3(8, 8, 4), 512, 0, stream>>>(
      (const u16*)d_in[0], (const u16*)d_in[10], (char*)d_ws, (const u16*)d_in[1], (const u16*)d_in[2], (const u16*)d_in[3],
      (const u16*)d_in[4], (const u16*)d_in[5], (const u16*)d_in[6], (const u16*)d_in[7], (const u16*)d_in[8],
      (const u16*)d_in[11], (const u16*)d_in[12], (const u16*)d_in[13], (u16*)d_out);
}
```
